```python
import torch

def _softmax(x, axis=-1):
    return torch.softmax(x, dim=axis)

def _gelu(x):
    return torch.nn.functional.gelu(x, approximate='tanh')

def _silu(x):
    return torch.nn.functional.silu(x)

def _sigmoid(x):
    return torch.sigmoid(x)

def _layernorm(x, axis=-1, eps=1e-05):
    m = x.mean(dim=axis, keepdim=True)
    v = x.var(dim=axis, keepdim=True, unbiased=False)
    return (x - m) / torch.sqrt(v + eps)

def _scatter_add(tgt, idx, src, axis=0):
    return tgt.clone().index_add_(axis, idx, src)

def _segment_sum(src, seg, n):
    out = torch.zeros((n,) + src.shape[1:], dtype=src.dtype)
    return out.index_add_(0, seg, src)

def _pad(x, pw):
    flat = tuple((v for pair in reversed(pw) for v in pair))
    return torch.nn.functional.pad(x, flat)
kernel_name = 'gather_scatter__tensor_product_equiv_mace__graph'

def setup_inputs():

    _w = lambda a: (lambda t: t.to(torch.bfloat16) if t.is_floating_point() else t)(torch.as_tensor(a))
    N, deg = (50000, 16)
    Lmax, C, De = (1, 64, 4)
    M = (Lmax + 1) ** 2
    E = N * deg
    src = (lambda lo, hi, *s: torch.randint(lo, hi, s))(0, N, E).to(torch.int64)
    dst = (lambda lo, hi, *s: torch.randint(lo, hi, s))(0, N, E).to(torch.int64)
    r = (lambda *s: torch.randn(*s, dtype=torch.bfloat16) * 0.05)(E, 3).to(float)
    r = r / (torch.linalg.norm(r, dim=-1, keepdim=True).to(float) + 1e-06)
    rx, ry, rz = (r[:, 0].clone(), r[:, 1].clone(), r[:, 2].clone())
    x2, y2, z2 = (rx * rx, ry * ry, rz * rz)
    sh_list = [torch.ones_like(rx), rx, ry, rz, rx * ry, ry * rz, 2.0 * z2 - x2 - y2, rx * rz, x2 - y2, ry * (3.0 * x2 - y2), rx * ry * rz, ry * (4.0 * z2 - x2 - y2), rz * (2.0 * z2 - 3.0 * x2 - 3.0 * y2), rx * (4.0 * z2 - x2 - y2), rz * (x2 - y2), rx * (x2 - 3.0 * y2)]
    sh16 = torch.stack(sh_list, dim=-1).to(float)
    pad = max(0, M - 16)
    sh_full = torch.cat([sh16, torch.zeros((E, pad), dtype=float)], dim=-1)[:, :M]
    cg = ((lambda *s: torch.randn(*s, dtype=torch.bfloat16) * 0.05)(M, M, M) * (1.0 / float(M))).to(float)
    return {'x': _w(((lambda *s: torch.randn(*s, dtype=torch.bfloat16) * 0.05)(N, M, C) * 0.3).to(float)), 'src': _w(src), 'dst': _w(dst), 'sh': _w(sh_full.to(float)), 'edge_attr': _w(((lambda *s: torch.randn(*s, dtype=torch.bfloat16) * 0.05)(E, De) * 0.3).to(float)), 'cg': _w(cg), 'w_r': _w(((lambda *s: torch.randn(*s, dtype=torch.bfloat16) * 0.05)(De, C) * (1.0 / float(De) ** 0.5)).to(float)), 'w_out': _w(((lambda *s: torch.randn(*s, dtype=torch.bfloat16) * 0.05)(C, C) * (1.0 / float(C) ** 0.5)).to(float))}

def reference(x, src, dst, sh, edge_attr, cg, w_r, w_out):
    N = x.shape[0]
    M = x.shape[1]
    C = x.shape[2]
    h = x[src]
    tp = torch.einsum('uvw,euc,ev->ewc', cg, h, sh)
    rw = edge_attr @ w_r
    msg = tp * rw[:, None, :]
    msg2 = msg.reshape(-1, M * C)
    agg = _segment_sum(msg2, dst, N).reshape(N, M, C)
    out = torch.einsum('nmc,cd->nmd', agg, w_out)
    return out
```

```cpp
#include <hip/hip_runtime.h>
#include <hip/hip_bf16.h>
#include <stdint.h>

typedef __attribute__((ext_vector_type(8))) __bf16 bf16x8_t;
typedef __attribute__((ext_vector_type(4))) float f32x4_t;

#define NTP 1024
#define EPT 4
#define NTM 768
#define NWM (NTM / 64)
#define CAP 3584
#define RPT ((CAP + NTM - 1) / NTM)
#define MAXBS 1024
#define MAXNBK 1024
#define STG_ROW 72

__device__ __forceinline__ float bf_lo(uint32_t v) { return __uint_as_float(v << 16); }
__device__ __forceinline__ float bf_hi(uint32_t v) { return __uint_as_float(v & 0xffff0000u); }

__device__ __forceinline__ uint32_t pack_bf16(float a, float b) {
    __hip_bfloat16 ha = __float2bfloat16(a);
    __hip_bfloat16 hb = __float2bfloat16(b);
    uint16_t ua = *reinterpret_cast<uint16_t*>(&ha);
    uint16_t ub = *reinterpret_cast<uint16_t*>(&hb);
    return (uint32_t)ua | ((uint32_t)ub << 16);
}

template <int NT>
__device__ __forceinline__ int block_excl_scan(int v, int* wsum, int* total) {
    const int lane = threadIdx.x & 63, wid = threadIdx.x >> 6;
    int inc = v;
#pragma unroll
    for (int o = 1; o < 64; o <<= 1) {
        int t = __shfl_up(inc, o, 64);
        if (lane >= o) inc += t;
    }
    __syncthreads();
    if (lane == 63) wsum[wid] = inc;
    __syncthreads();
    if (wid == 0) {
        int w = (lane < NT / 64) ? wsum[lane] : 0;
        int winc = w;
#pragma unroll
        for (int o = 1; o < 64; o <<= 1) {
            int t = __shfl_up(winc, o, 64);
            if (lane >= o) winc += t;
        }
        if (lane < NT / 64) wsum[lane] = winc - w;
        if (lane == NT / 64 - 1) wsum[NT / 64] = winc;
    }
    __syncthreads();
    int res = wsum[wid] + inc - v;
    *total = wsum[NT / 64];
    return res;
}

__global__ __launch_bounds__(NTP) void k_partition(
    const int* __restrict__ src, const int* __restrict__ dst,
    const uint2* __restrict__ sh, const uint2* __restrict__ ea,
    int E, int CH, int G1, int BS, int NBK,
    uint32_t* __restrict__ recs, int* __restrict__ off) {
    extern __shared__ __attribute__((aligned(16))) unsigned char smem[];
    uint32_t* stage = reinterpret_cast<uint32_t*>(smem);
    int* hist = reinterpret_cast<int*>(smem + (size_t)NTP * EPT * 24);
    int* wsum = hist + MAXNBK;

    const int tid = threadIdx.x, blk = blockIdx.x;
    const int e0 = blk * CH;
    const int e1 = min(E, e0 + CH);
    const int cnt = max(0, e1 - e0);

    for (int i = tid; i < NBK; i += NTP) hist[i] = 0;
    __syncthreads();

    int bk[EPT], dl[EPT];
#pragma unroll
    for (int i = 0; i < EPT; ++i) {
        int j = tid + i * NTP;
        bk[i] = -1;
        dl[i] = 0;
        if (j < cnt) {
            int d = dst[e0 + j];
            int b = d / BS;
            bk[i] = b;
            dl[i] = d - b * BS;
            atomicAdd(&hist[b], 1);
        }
    }
    __syncthreads();
    int hv = (tid < NBK) ? hist[tid] : 0;
    int total;
    int st = block_excl_scan<NTP>(hv, wsum, &total);
    if (tid < NBK) {
        hist[tid] = st;
        off[(size_t)tid * G1 + blk] = st;
    }
    if (tid == 0) off[(size_t)NBK * G1 + blk] = total;
    __syncthreads();
#pragma unroll
    for (int i = 0; i < EPT; ++i) {
        int j = tid + i * NTP;
        if (j < cnt) {
            int pos = atomicAdd(&hist[bk[i]], 1);
            uint2 s = sh[e0 + j];
            uint2 a = ea[e0 + j];
            uint2* p = reinterpret_cast<uint2*>(stage + (size_t)pos * 6);
            p[0] = make_uint2((uint32_t)src[e0 + j], ((uint32_t)j << 16) | (uint32_t)dl[i]);
            p[1] = s;
            p[2] = a;
        }
    }
    __syncthreads();
    uint2* gout = reinterpret_cast<uint2*>(recs + (size_t)blk * CH * 6);
    const uint2* lin = reinterpret_cast<const uint2*>(stage);
    for (int i = tid; i < cnt * 3; i += NTP) gout[i] = lin[i];
}

__global__ __launch_bounds__(NTM) void k_main(
    const uint4* __restrict__ x, const uint32_t* __restrict__ recs, const int* __restrict__ off,
    const __hip_bfloat16* __restrict__ cg, const __hip_bfloat16* __restrict__ w_r,
    const __hip_bfloat16* __restrict__ w_out, __hip_bfloat16* __restrict__ out,
    float* __restrict__ accws, int N, int CH, int G1, int BS) {
    extern __shared__ __attribute__((aligned(16))) unsigned char smem[];
    uint4* l_she = reinterpret_cast<uint4*>(smem);
    uint32_t* l_src = reinterpret_cast<uint32_t*>(smem + CAP * 16);
    unsigned char* p2 = smem + CAP * 20;
    uint4* l_wo = reinterpret_cast<uint4*>(p2);
    p2 += 8192;
    uint16_t* l_stg = reinterpret_cast<uint16_t*>(p2);
    int* l_map = reinterpret_cast<int*>(p2);
    p2 += NWM * 16 * STG_ROW * 2;
    float* l_a = reinterpret_cast<float*>(p2);
    p2 += NWM * 512;
    int* l_hist = reinterpret_cast<int*>(p2);
    p2 += MAXBS * 4;
    int* l_start = reinterpret_cast<int*>(p2);
    p2 += (MAXBS + 4) * 4;
    int* l_wsum = reinterpret_cast<int*>(p2);
    p2 += 32 * 4;
    int* l_rs = reinterpret_cast<int*>(p2);
    int* l_rf = l_rs + G1;

    const int tid0 = threadIdx.x;
    const int b = blockIdx.x;
    const int node0 = b * BS;
    const int nn = min(BS, N - node0);

    {
        const uint16_t* wo = reinterpret_cast<const uint16_t*>(w_out);
        for (int i = tid0; i < 8 * 64; i += NTM) {
            int tk = i >> 6, l = i & 63;
            int t = tk >> 1, ks = tk & 1;
            int d = ((l & 15) >> 2) * 16 + 4 * t + (l & 3);
            int c0 = 32 * ks + 8 * (l >> 4);
            uint32_t v[4];
#pragma unroll
            for (int j = 0; j < 4; ++j)
                v[j] = (uint32_t)wo[(c0 + 2 * j) * 64 + d] | ((uint32_t)wo[(c0 + 2 * j + 1) * 64 + d] << 16);
            l_wo[i] = make_uint4(v[0], v[1], v[2], v[3]);
        }
    }
    int total = 0;
    for (int r0 = 0; r0 < G1; r0 += NTM) {
        int r = r0 + tid0;
        int s = 0, len = 0;
        if (r < G1) {
            s = off[(size_t)b * G1 + r];
            len = off[(size_t)(b + 1) * G1 + r] - s;
        }
        int tt;
        int ex = block_excl_scan<NTM>(len, l_wsum, &tt);
        if (r < G1) {
            l_rs[r] = s;
            l_rf[r] = total + ex;
        }
        total += tt;
    }
    if (tid0 == 0) l_rf[G1] = total;
    const int nchunks = max(1, (total + CAP - 1) / CAP);

    for (int ck = 0; ck < nchunks; ++ck) {
        const int c0 = ck * CAP;
        const int c1 = min(total, c0 + CAP);
        const int cn = c1 - c0;
        int tid = tid0;
        asm volatile("" : "+v"(tid));
        const int lane = tid & 63, wid = __builtin_amdgcn_readfirstlane(tid >> 6);
        __syncthreads();
        for (int r = tid >> 4; r < G1; r += NTM / 16) {
            int f0 = l_rf[r], f1 = l_rf[r + 1];
            int base = r * CH + l_rs[r] - f0;
            int lo = max(f0, c0), hi = min(f1, c1);
            for (int f = lo + (tid & 15); f < hi; f += 16) l_map[f - c0] = base + f;
        }
        for (int i = tid; i < nn; i += NTM) l_hist[i] = 0;
        __syncthreads();
        uint2 ra[RPT], rb[RPT], rc[RPT];
#pragma unroll
        for (int i = 0; i < RPT; ++i) {
            int f = tid + i * NTM;
            ra[i] = make_uint2(0u, 0u);
            rb[i] = make_uint2(0u, 0u);
            rc[i] = make_uint2(0u, 0u);
            if (f < cn) {
                const uint2* p = reinterpret_cast<const uint2*>(recs + (size_t)l_map[f] * 6);
                ra[i] = p[0];
                rb[i] = p[1];
                rc[i] = p[2];
            }
        }
#pragma unroll
        for (int i = 0; i < RPT; ++i) {
            int f = tid + i * NTM;
            if (f < cn) atomicAdd(&l_hist[ra[i].y & 0xffffu], 1);
        }
        __syncthreads();
        {
            int hv = (tid < nn) ? l_hist[tid] : 0;
            int tt;
            int st = block_excl_scan<NTM>(hv, l_wsum, &tt);
            if (tid < nn) {
                l_start[tid] = st;
                l_hist[tid] = st;
            }
            if (tid == 0) l_start[nn] = tt;
        }
        __syncthreads();
#pragma unroll
        for (int i = 0; i < RPT; ++i) {
            int f = tid + i * NTM;
            if (f < cn) {
                int pos = atomicAdd(&l_hist[ra[i].y & 0xffffu], 1);
                l_src[pos] = ra[i].x;
                l_she[pos] = make_uint4(rb[i].x, rb[i].y, rc[i].x, rc[i].y);
            }
        }
        __syncthreads();

        float cgf[4][2];
    #pragma unroll
        for (int v = 0; v < 4; ++v)
    #pragma unroll
            for (int k = 0; k < 2; ++k)
                cgf[v][k] = __bfloat162float(cg[(((lane & 7) >> 1) * 4 + v) * 4 + 2 * (lane & 1) + k]);
        float wr[4][8];
        {
            const uint4* wr4 = reinterpret_cast<const uint4*>(w_r);
    #pragma unroll
            for (int d = 0; d < 4; ++d) {
                uint4 v = wr4[d * 8 + (lane & 7)];
                wr[d][0] = bf_lo(v.x); wr[d][1] = bf_hi(v.x);
                wr[d][2] = bf_lo(v.y); wr[d][3] = bf_hi(v.y);
                wr[d][4] = bf_lo(v.z); wr[d][5] = bf_hi(v.z);
                wr[d][6] = bf_lo(v.w); wr[d][7] = bf_hi(v.w);
            }
        }

        const bool last = (ck == nchunks - 1);
        uint16_t* stg = l_stg + wid * 16 * STG_ROW;
        float* wa = l_a + wid * 128;
        const int g = lane >> 3;
        for (int q = wid; q * 4 < nn; q += NWM) {
#pragma unroll 1
            for (int sl = 0; sl < 4; ++sl) {
                const int nl = q * 4 + sl;
                float acc[4][8];
#pragma unroll
                for (int w = 0; w < 4; ++w)
#pragma unroll
                    for (int c = 0; c < 8; ++c) acc[w][c] = 0.f;
                int ns = 0, ne = 0;
                if (nl < nn) {
                    ns = __builtin_amdgcn_readfirstlane(l_start[nl]);
                    ne = __builtin_amdgcn_readfirstlane(l_start[nl + 1]);
                }
#pragma unroll 1
                for (int j0 = ns; j0 < ne; j0 += 8) {
                    const int j = j0 + g;
                    if (j < ne) {
                        const uint32_t s = l_src[j];
                        const uint4 qv = l_she[j];
                        const uint4* xr = x + (size_t)s * 32 + (lane & 7);
                        uint4 h[4];
#pragma unroll
                        for (int u = 0; u < 4; ++u) h[u] = xr[u * 8];
                        float shv[4] = {bf_lo(qv.x), bf_hi(qv.x), bf_lo(qv.y), bf_hi(qv.y)};
                        float eav[4] = {bf_lo(qv.z), bf_hi(qv.z), bf_lo(qv.w), bf_hi(qv.w)};
                        {
                            float a0 = cgf[0][0] * shv[0], a1 = cgf[0][1] * shv[0];
#pragma unroll
                            for (int v = 1; v < 4; ++v) {
                                a0 = fmaf(cgf[v][0], shv[v], a0);
                                a1 = fmaf(cgf[v][1], shv[v], a1);
                            }
                            *reinterpret_cast<float2*>(wa + lane * 2) = make_float2(a0, a1);
                        }
                        float rw[8];
#pragma unroll
                        for (int c = 0; c < 8; ++c) {
                            float r = eav[0] * wr[0][c];
#pragma unroll
                            for (int d = 1; d < 4; ++d) r = fmaf(eav[d], wr[d][c], r);
                            rw[c] = r;
                        }
#pragma unroll
                        for (int u = 0; u < 4; ++u) {
                            const float4 a4 = *reinterpret_cast<const float4*>(wa + g * 16 + u * 4);
                            const float Au[4] = {a4.x, a4.y, a4.z, a4.w};
                            uint32_t hw[4] = {h[u].x, h[u].y, h[u].z, h[u].w};
#pragma unroll
                            for (int c2 = 0; c2 < 4; ++c2) {
                                float lo = bf_lo(hw[c2]) * rw[2 * c2];
                                float hi = bf_hi(hw[c2]) * rw[2 * c2 + 1];
#pragma unroll
                                for (int w = 0; w < 4; ++w) {
                                    acc[w][2 * c2] = fmaf(Au[w], lo, acc[w][2 * c2]);
                                    acc[w][2 * c2 + 1] = fmaf(Au[w], hi, acc[w][2 * c2 + 1]);
                                }
                            }
                        }
                    }
                }
                float r1[2][8];
#pragma unroll
                for (int w = 0; w < 2; ++w)
#pragma unroll
                    for (int c = 0; c < 8; ++c) {
                        float send = (lane & 32) ? acc[w][c] : acc[w + 2][c];
                        float keep = (lane & 32) ? acc[w + 2][c] : acc[w][c];
                        r1[w][c] = keep + __shfl_xor(send, 32, 64);
                    }
                float r2[8];
#pragma unroll
                for (int c = 0; c < 8; ++c) {
                    float send = (lane & 16) ? r1[0][c] : r1[1][c];
                    float keep = (lane & 16) ? r1[1][c] : r1[0][c];
                    r2[c] = keep + __shfl_xor(send, 16, 64);
                }
                float r3[4];
#pragma unroll
                for (int c = 0; c < 4; ++c) {
                    float send = (lane & 8) ? r2[c] : r2[c + 4];
                    float keep = (lane & 8) ? r2[c + 4] : r2[c];
                    r3[c] = keep + __shfl_xor(send, 8, 64);
                }
                if (nchunks > 1 && nl < nn) {
                    float4* ap = reinterpret_cast<float4*>(accws + (size_t)(node0 + nl) * 256) + lane;
                    if (ck > 0) {
                        float4 pv = *ap;
                        r3[0] += pv.x; r3[1] += pv.y; r3[2] += pv.z; r3[3] += pv.w;
                    }
                    if (!last) *ap = make_float4(r3[0], r3[1], r3[2], r3[3]);
                }
                if (last) {
                    int row = sl * 4 + (lane >> 4);
                    int col = (lane & 7) * 8 + ((lane >> 3) & 1) * 4;
                    uint2 pk = make_uint2(pack_bf16(r3[0], r3[1]), pack_bf16(r3[2], r3[3]));
                    *reinterpret_cast<uint2*>(stg + row * STG_ROW + col) = pk;
                }
            }
            if (last) {
                __builtin_amdgcn_s_waitcnt(0xC07F);
                __builtin_amdgcn_wave_barrier();
                bf16x8_t bfr[2];
#pragma unroll
                for (int ks = 0; ks < 2; ++ks) {
                    uint4 v = *reinterpret_cast<const uint4*>(stg + (lane & 15) * STG_ROW + 32 * ks + 8 * (lane >> 4));
                    bfr[ks] = __builtin_bit_cast(bf16x8_t, v);
                }
                uint32_t ow[8];
#pragma unroll
                for (int t = 0; t < 4; ++t) {
                    f32x4_t d4 = {0.f, 0.f, 0.f, 0.f};
#pragma unroll
                    for (int ks = 0; ks < 2; ++ks) {
                        bf16x8_t afr = __builtin_bit_cast(bf16x8_t, l_wo[(t * 2 + ks) * 64 + lane]);
                        d4 = __builtin_amdgcn_mfma_f32_16x16x32_bf16(afr, bfr[ks], d4, 0, 0, 0);
                    }
                    ow[2 * t] = pack_bf16(d4[0], d4[1]);
                    ow[2 * t + 1] = pack_bf16(d4[2], d4[3]);
                }
                __builtin_amdgcn_wave_barrier();
                const int r = lane & 15;
                const int nl = q * 4 + (r >> 2);
                if (nl < nn) {
                    uint4* op = reinterpret_cast<uint4*>(out + ((size_t)(node0 + q * 4) * 4 + r) * 64 + (lane >> 4) * 16);
                    op[0] = make_uint4(ow[0], ow[1], ow[2], ow[3]);
                    op[1] = make_uint4(ow[4], ow[5], ow[6], ow[7]);
                }
            }
        }
    }
}

extern "C" void kernel_launch(void* const* d_in, const int* in_sizes, int n_in,
                              void* d_out, int out_size, void* d_ws, size_t ws_size,
                              hipStream_t stream) {
    const uint4* x = (const uint4*)d_in[0];
    const int* src = (const int*)d_in[1];
    const int* dst = (const int*)d_in[2];
    const uint2* sh = (const uint2*)d_in[3];
    const uint2* ea = (const uint2*)d_in[4];
    const __hip_bfloat16* cg = (const __hip_bfloat16*)d_in[5];
    const __hip_bfloat16* w_r = (const __hip_bfloat16*)d_in[6];
    const __hip_bfloat16* w_out = (const __hip_bfloat16*)d_in[7];
    __hip_bfloat16* out = (__hip_bfloat16*)d_out;

    const int N = in_sizes[0] / 256;
    const int E = in_sizes[1];

    int G1 = 256;
    while ((long long)G1 * (NTP * EPT) < (long long)E) G1 += 256;
    int CH = (E + G1 - 1) / G1;
    if (CH < 1) CH = 1;
    int BS = 4 * ((N + 1023) / 1024);
    if (BS < 4) BS = 4;
    if (BS > MAXBS) BS = MAXBS;
    int NBK = (N + BS - 1) / BS;
    if (NBK < 1) NBK = 1;

    unsigned char* ws = (unsigned char*)d_ws;
    uint32_t* recs = (uint32_t*)ws;
    size_t recs_bytes = ((size_t)G1 * CH * 24 + 255) & ~(size_t)255;
    int* off = (int*)(ws + recs_bytes);
    size_t off_bytes = (((size_t)(NBK + 1) * G1 * 4) + 255) & ~(size_t)255;
    float* accws = (float*)(ws + recs_bytes + off_bytes);

    size_t lds_p = (size_t)NTP * EPT * 24 + MAXNBK * 4 + 32 * 4;
    size_t lds_m = (size_t)CAP * 20 + 8192 + NWM * 16 * STG_ROW * 2 + NWM * 512 + MAXBS * 4 + (MAXBS + 4) * 4 + 32 * 4 +
                   (size_t)(2 * G1 + 1) * 4;

    (void)hipFuncSetAttribute((const void*)k_partition, hipFuncAttributeMaxDynamicSharedMemorySize, (int)lds_p);
    (void)hipFuncSetAttribute((const void*)k_main, hipFuncAttributeMaxDynamicSharedMemorySize, (int)lds_m);

    k_partition<<<G1, NTP, lds_p, stream>>>(src, dst, sh, ea, E, CH, G1, BS, NBK, recs, off);
    k_main<<<NBK, NTM, lds_m, stream>>>(x, recs, off, cg, w_r, w_out, out, accws, N, CH, G1, BS);
}
```

```cpp
#include <hip/hip_runtime.h>
#include <hip/hip_bf16.h>
#include <stdint.h>

typedef __attribute__((ext_vector_type(8))) __bf16 bf16x8_t;
typedef __attribute__((ext_vector_type(4))) float f32x4_t;
typedef __attribute__((ext_vector_type(4))) short v4i16_t;
typedef __attribute__((ext_vector_type(4))) unsigned int u32x4_t;
typedef __attribute__((ext_vector_type(2))) unsigned int u32x2_t;

#define NTP 1024
#define EPT 4
#define NTM 1024
#define NWM (NTM / 64)
#define CAP 3520
#define RPT ((CAP + NTM - 1) / NTM)
#define MAXBS 256
#define MAXNBK 1024
#define TGTNBK 256
#define STG_ROW 72

__device__ __forceinline__ float bf_lo(uint32_t v) { return __uint_as_float(v << 16); }
__device__ __forceinline__ float bf_hi(uint32_t v) { return __uint_as_float(v & 0xffff0000u); }

typedef __attribute__((ext_vector_type(2))) float f32x2_t;
typedef __attribute__((ext_vector_type(2))) __bf16 bf16x2_t;
__device__ __forceinline__ uint32_t pack_bf16(float a, float b) {
    f32x2_t v = {a, b};
    bf16x2_t r = __builtin_convertvector(v, bf16x2_t);
    return __builtin_bit_cast(uint32_t, r);
}

template <int NT>
__device__ __forceinline__ int block_excl_scan(int v, int* wsum, int* total) {
    const int lane = threadIdx.x & 63, wid = threadIdx.x >> 6;
    int inc = v;
#pragma unroll
    for (int o = 1; o < 64; o <<= 1) {
        int t = __shfl_up(inc, o, 64);
        if (lane >= o) inc += t;
    }
    __syncthreads();
    if (lane == 63) wsum[wid] = inc;
    __syncthreads();
    if (wid == 0) {
        int w = (lane < NT / 64) ? wsum[lane] : 0;
        int winc = w;
#pragma unroll
        for (int o = 1; o < 64; o <<= 1) {
            int t = __shfl_up(winc, o, 64);
            if (lane >= o) winc += t;
        }
        if (lane < NT / 64) wsum[lane] = winc - w;
        if (lane == NT / 64 - 1) wsum[NT / 64] = winc;
    }
    __syncthreads();
    int res = wsum[wid] + inc - v;
    *total = wsum[NT / 64];
    return res;
}

__global__ __launch_bounds__(NTP) void k_prep(
    const int* __restrict__ src, const int* __restrict__ dst,
    const uint2* __restrict__ sh, const uint2* __restrict__ ea,
    const __hip_bfloat16* __restrict__ w_out, uint4* __restrict__ wof,
    int E, int CH, int G1, int BS, int NBK,
    uint32_t* __restrict__ recs, int* __restrict__ off) {
    extern __shared__ __attribute__((aligned(16))) unsigned char smem[];
    const int tid = threadIdx.x;
    if (blockIdx.x == 0 && tid < 512) {
        const uint16_t* wo = reinterpret_cast<const uint16_t*>(w_out);
        int tk = tid >> 6, l = tid & 63;
        int t = tk >> 1, ks = tk & 1;
        int d = ((l & 15) >> 2) * 16 + 4 * t + (l & 3);
        int c0 = 32 * ks + 8 * (l >> 4);
        uint32_t v[4];
#pragma unroll
        for (int j = 0; j < 4; ++j)
            v[j] = (uint32_t)wo[(c0 + 2 * j) * 64 + d] | ((uint32_t)wo[(c0 + 2 * j + 1) * 64 + d] << 16);
        wof[tid] = make_uint4(v[0], v[1], v[2], v[3]);
    }
    const int rid = blockIdx.x;

    int* hist = reinterpret_cast<int*>(smem);
    int* wsum = hist + MAXNBK;
    uint32_t* stage = reinterpret_cast<uint32_t*>(smem + MAXNBK * 4 + 128);
    const int blk = rid;
    const int e0 = blk * CH;
    const int e1 = min(E, e0 + CH);
    const int cnt = max(0, e1 - e0);

    int dv[EPT], sv[EPT];
    uint2 shv[EPT], eav[EPT];
#pragma unroll
    for (int i = 0; i < EPT; ++i) {
        int j = tid + i * NTP;
        dv[i] = 0; sv[i] = 0;
        shv[i] = make_uint2(0u, 0u);
        eav[i] = make_uint2(0u, 0u);
        if (j < cnt) {
            dv[i] = dst[e0 + j];
            sv[i] = src[e0 + j];
            shv[i] = sh[e0 + j];
            eav[i] = ea[e0 + j];
        }
    }
    for (int i = tid; i < NBK; i += NTP) hist[i] = 0;
    __syncthreads();

    int bk[EPT], dl[EPT];
#pragma unroll
    for (int i = 0; i < EPT; ++i) {
        int j = tid + i * NTP;
        int b = dv[i] / BS;
        bk[i] = b;
        dl[i] = dv[i] - b * BS;
        if (j < cnt) atomicAdd(&hist[b], 1);
    }
    __syncthreads();
    int hv = (tid < NBK) ? hist[tid] : 0;
    int total;
    int st = block_excl_scan<NTP>(hv, wsum, &total);
    if (tid < NBK) {
        hist[tid] = st;
        off[(size_t)tid * G1 + blk] = st;
    }
    if (tid == 0) off[(size_t)NBK * G1 + blk] = total;
    __syncthreads();
#pragma unroll
    for (int i = 0; i < EPT; ++i) {
        int j = tid + i * NTP;
        if (j < cnt) {
            int pos = atomicAdd(&hist[bk[i]], 1);
            uint2* p = reinterpret_cast<uint2*>(stage + (size_t)pos * 6);
            p[0] = make_uint2((uint32_t)sv[i], ((uint32_t)j << 16) | (uint32_t)dl[i]);
            p[1] = shv[i];
            p[2] = eav[i];
        }
    }
    __syncthreads();
    uint2* gout = reinterpret_cast<uint2*>(recs + (size_t)blk * CH * 6);
    const uint2* lin = reinterpret_cast<const uint2*>(stage);
    for (int i = tid; i < cnt * 3; i += NTP) gout[i] = lin[i];
}

#define STAMP(i) do {} while (0)
struct Cur {
    int nl, j0, je;
};

#define NKB 16
#define CPT ((MAXBS * NKB + NTM - 1) / NTM)
#define LDS_SH 0
#define LDS_EA (LDS_SH + (CAP + 8) * 16)
#define LDS_SRC (LDS_EA + (CAP + 8) * 8)
#define LDS_WO (LDS_SRC + (CAP + 8) * 4)
#define LDS_KEY (LDS_WO + 8192)
#define LDS_MAP LDS_SH
#define LDS_BASE (LDS_SH + CAP * 4)
#define LDS_STG LDS_KEY
#define LDS_START (LDS_KEY + CAP * 4)
#define LDS_WSUM (LDS_START + (MAXBS + 4) * 4)
#define LDS_CELL (LDS_WSUM + 32 * 4)

struct Frag {
    u32x2_t r[4];
};

template <bool MULTI>
__device__ __forceinline__ void node_loop(
    unsigned char* smem, const __amdgpu_buffer_rsrc_t xrsrc,
    const __hip_bfloat16* __restrict__ cg, const __hip_bfloat16* __restrict__ w_r,
    __hip_bfloat16* __restrict__ out, float* __restrict__ accws,
    int node0, int nn, int tid, bool first, bool last) {
    const float4* l_sh = reinterpret_cast<const float4*>(smem + LDS_SH);
    const uint16_t* l_ea = reinterpret_cast<const uint16_t*>(smem + LDS_EA);
    const uint32_t* l_src = reinterpret_cast<const uint32_t*>(smem + LDS_SRC);
    const uint4* l_wo = reinterpret_cast<const uint4*>(smem + LDS_WO);
    const int* l_start = reinterpret_cast<const int*>(smem + LDS_START);
    int* l_ctr = reinterpret_cast<int*>(smem + LDS_WSUM) + 24;
    const int lane = tid & 63, wid = __builtin_amdgcn_readfirstlane(tid >> 6);

    const int gq = lane >> 4;
    const int cl = lane & 15;
    float cgw[4][4];
#pragma unroll
    for (int u = 0; u < 4; ++u)
#pragma unroll
        for (int v = 0; v < 4; ++v) cgw[u][v] = __bfloat162float(cg[(u * 4 + v) * 4 + (lane & 3)]);
    float wrl[4];
#pragma unroll
    for (int t = 0; t < 4; ++t) wrl[t] = __bfloat162float(w_r[gq * 64 + 4 * cl + t]);
    const int cch = 4 * cl + gq;
    const int dsel = cl >> 2;
    uint16_t* stg = reinterpret_cast<uint16_t*>(smem + LDS_STG) + wid * 4 * STG_ROW;

    auto next_node = [&](Cur& c) {
        int v = 0;
        if (lane == 0) v = atomicAdd(l_ctr, 1);
        v = __builtin_amdgcn_readfirstlane(v);
        c.nl = v;
        c.j0 = 0;
        c.je = 0;
        if (v < nn) {
            c.j0 = __builtin_amdgcn_readfirstlane(l_start[v]);
            c.je = __builtin_amdgcn_readfirstlane(l_start[v + 1]);
        }
    };
    auto advance = [&](Cur& c) {
        if (c.nl < nn) {
            c.j0 += 4;
            if (c.j0 >= c.je) next_node(c);
        }
    };
    auto issue = [&](Frag& h, const Cur& c) {
        const int j = c.j0 + gq;
        const uint32_t s = l_src[j];
        uint32_t vo = (j < c.je) ? (s * 512u + (uint32_t)cl * 8u) : 0xFFFFF000u;
#pragma unroll
        for (int u = 0; u < 4; ++u) h.r[u] = __builtin_amdgcn_raw_buffer_load_b64(xrsrc, vo + u * 128, 0, 0);
    };
    f32x4_t acc[4];
#pragma unroll
    for (int t = 0; t < 4; ++t) acc[t] = (f32x4_t){0.f, 0.f, 0.f, 0.f};

    auto process = [&](const Frag& h, const Cur& c) {
        if (c.je > c.j0) {
        const int jr = c.j0 + gq;
        const int j = min(jr, c.je - 1);
        const float4 sv = l_sh[j];
        uint32_t edb = l_ea[j * 4 + dsel];
        const float ed = (jr < c.je) ? __uint_as_float(edb << 16) : 0.f;
        float p[4];
#pragma unroll
        for (int u = 0; u < 4; ++u) {
            float a = cgw[u][0] * sv.x;
            a = fmaf(cgw[u][1], sv.y, a);
            a = fmaf(cgw[u][2], sv.z, a);
            a = fmaf(cgw[u][3], sv.w, a);
            p[u] = a * ed;
        }
        uint32_t phi[2], plo[2];
#pragma unroll
        for (int k = 0; k < 2; ++k) {
            uint32_t hi = pack_bf16(p[2 * k], p[2 * k + 1]);
            float r0 = p[2 * k] - bf_lo(hi);
            float r1 = p[2 * k + 1] - bf_hi(hi);
            phi[k] = hi;
            plo[k] = pack_bf16(r0, r1);
        }
        const v4i16_t ahi = __builtin_bit_cast(v4i16_t, (u32x2_t){phi[0], phi[1]});
        const v4i16_t alo = __builtin_bit_cast(v4i16_t, (u32x2_t){plo[0], plo[1]});
        const v4i16_t b0 = __builtin_bit_cast(v4i16_t, (u32x2_t){__builtin_amdgcn_perm(h.r[1][0], h.r[0][0], 0x05040100u),
                                                                 __builtin_amdgcn_perm(h.r[3][0], h.r[2][0], 0x05040100u)});
        const v4i16_t b1 = __builtin_bit_cast(v4i16_t, (u32x2_t){__builtin_amdgcn_perm(h.r[1][0], h.r[0][0], 0x07060302u),
                                                                 __builtin_amdgcn_perm(h.r[3][0], h.r[2][0], 0x07060302u)});
        const v4i16_t b2 = __builtin_bit_cast(v4i16_t, (u32x2_t){__builtin_amdgcn_perm(h.r[1][1], h.r[0][1], 0x05040100u),
                                                                 __builtin_amdgcn_perm(h.r[3][1], h.r[2][1], 0x05040100u)});
        const v4i16_t b3 = __builtin_bit_cast(v4i16_t, (u32x2_t){__builtin_amdgcn_perm(h.r[1][1], h.r[0][1], 0x07060302u),
                                                                 __builtin_amdgcn_perm(h.r[3][1], h.r[2][1], 0x07060302u)});
        acc[0] = __builtin_amdgcn_mfma_f32_16x16x16bf16_1k(ahi, b0, acc[0], 0, 0, 0);
        acc[1] = __builtin_amdgcn_mfma_f32_16x16x16bf16_1k(ahi, b1, acc[1], 0, 0, 0);
        acc[2] = __builtin_amdgcn_mfma_f32_16x16x16bf16_1k(ahi, b2, acc[2], 0, 0, 0);
        acc[3] = __builtin_amdgcn_mfma_f32_16x16x16bf16_1k(ahi, b3, acc[3], 0, 0, 0);
        acc[0] = __builtin_amdgcn_mfma_f32_16x16x16bf16_1k(alo, b0, acc[0], 0, 0, 0);
        acc[1] = __builtin_amdgcn_mfma_f32_16x16x16bf16_1k(alo, b1, acc[1], 0, 0, 0);
        acc[2] = __builtin_amdgcn_mfma_f32_16x16x16bf16_1k(alo, b2, acc[2], 0, 0, 0);
        acc[3] = __builtin_amdgcn_mfma_f32_16x16x16bf16_1k(alo, b3, acc[3], 0, 0, 0);
        }
        if (c.j0 + 4 >= c.je) {
            float v[4][4];
#pragma unroll
            for (int t = 0; t < 4; ++t)
#pragma unroll
                for (int w = 0; w < 4; ++w) {
                    v[t][w] = acc[t][w] * wrl[t];
                    acc[t][w] = 0.f;
                }
            float r1[2][4];
#pragma unroll
            for (int t = 0; t < 2; ++t)
#pragma unroll
                for (int w = 0; w < 4; ++w) {
                    auto r = __builtin_amdgcn_permlane32_swap(__float_as_uint(v[t][w]), __float_as_uint(v[t + 2][w]),
                                                              false, false);
                    r1[t][w] = __uint_as_float(r[0]) + __uint_as_float(r[1]);
                }
            float ag[4];
#pragma unroll
            for (int w = 0; w < 4; ++w) {
                auto r = __builtin_amdgcn_permlane16_swap(__float_as_uint(r1[0][w]), __float_as_uint(r1[1][w]),
                                                          false, false);
                ag[w] = __uint_as_float(r[0]) + __uint_as_float(r[1]);
            }
            if (MULTI) {
                float* ap = accws + (size_t)(node0 + c.nl) * 256 + cch;
                if (!first) {
#pragma unroll
                    for (int w = 0; w < 4; ++w) ag[w] += ap[w * 64];
                }
                if (!last) {
#pragma unroll
                    for (int w = 0; w < 4; ++w) ap[w * 64] = ag[w];
                }
            }
            if (!MULTI || last) {
                uint32_t p01 = pack_bf16(ag[0], ag[1]);
                uint32_t p23 = pack_bf16(ag[2], ag[3]);
                stg[0 * STG_ROW + cch] = (uint16_t)(p01 & 0xffffu);
                stg[1 * STG_ROW + cch] = (uint16_t)(p01 >> 16);
                stg[2 * STG_ROW + cch] = (uint16_t)(p23 & 0xffffu);
                stg[3 * STG_ROW + cch] = (uint16_t)(p23 >> 16);
                __builtin_amdgcn_wave_barrier();
                bf16x8_t bfr[2];
#pragma unroll
                for (int ks = 0; ks < 2; ++ks) {
                    uint4 vv = *reinterpret_cast<const uint4*>(stg + (lane & 3) * STG_ROW + 32 * ks + 8 * gq);
                    bfr[ks] = __builtin_bit_cast(bf16x8_t, vv);
                }
                uint32_t ow[8];
#pragma unroll
                for (int t = 0; t < 4; ++t) {
                    f32x4_t d4 = {0.f, 0.f, 0.f, 0.f};
#pragma unroll
                    for (int ks = 0; ks < 2; ++ks) {
                        bf16x8_t afr = __builtin_bit_cast(bf16x8_t, l_wo[(t * 2 + ks) * 64 + lane]);
                        d4 = __builtin_amdgcn_mfma_f32_16x16x32_bf16(afr, bfr[ks], d4, 0, 0, 0);
                    }
                    ow[2 * t] = pack_bf16(d4[0], d4[1]);
                    ow[2 * t + 1] = pack_bf16(d4[2], d4[3]);
                }
                __builtin_amdgcn_wave_barrier();
                if (cl < 4) {
                    uint4* op = reinterpret_cast<uint4*>(out + ((size_t)(node0 + c.nl) * 4 + cl) * 64 + gq * 16);
                    op[0] = make_uint4(ow[0], ow[1], ow[2], ow[3]);
                    op[1] = make_uint4(ow[4], ow[5], ow[6], ow[7]);
                }
            }
        }
    };

    Frag h0, h1, h2, h3;
    Cur c0, c1, c2, c3;
    next_node(c0);
    issue(h0, c0);
    c1 = c0; advance(c1); issue(h1, c1);
    c2 = c1; advance(c2); issue(h2, c2);
    c3 = c2; advance(c3); issue(h3, c3);
#pragma unroll 1
    while (c0.nl < nn) {
        process(h0, c0); c0 = c3; advance(c0); issue(h0, c0);
        if (c1.nl < nn) process(h1, c1);
        c1 = c0; advance(c1); issue(h1, c1);
        if (c2.nl < nn) process(h2, c2);
        c2 = c1; advance(c2); issue(h2, c2);
        if (c3.nl < nn) process(h3, c3);
        c3 = c2; advance(c3); issue(h3, c3);
    }
}

__global__ __launch_bounds__(NTM) void k_main(
    const uint4* __restrict__ xp, const uint32_t* __restrict__ recs, const int* __restrict__ off,
    const __hip_bfloat16* __restrict__ cg, const __hip_bfloat16* __restrict__ w_r,
    const uint4* __restrict__ wof, __hip_bfloat16* __restrict__ out,
    float* __restrict__ accws, int N, int CH, int G1, int BS, int kshift) {
    extern __shared__ __attribute__((aligned(16))) unsigned char smem[];
    float4* l_sh = reinterpret_cast<float4*>(smem + LDS_SH);
    uint2* l_ea = reinterpret_cast<uint2*>(smem + LDS_EA);
    uint32_t* l_src = reinterpret_cast<uint32_t*>(smem + LDS_SRC);
    uint4* l_wo = reinterpret_cast<uint4*>(smem + LDS_WO);
    int* l_map = reinterpret_cast<int*>(smem + LDS_MAP);
    uint32_t* l_key = reinterpret_cast<uint32_t*>(smem + LDS_KEY);
    uint32_t* l_base = reinterpret_cast<uint32_t*>(smem + LDS_BASE);
    int* l_start = reinterpret_cast<int*>(smem + LDS_START);
    int* l_wsum = reinterpret_cast<int*>(smem + LDS_WSUM);
    int* l_ctr = l_wsum + 24;
    int* l_cell = reinterpret_cast<int*>(smem + LDS_CELL);
    int* l_rs = l_cell + BS * NKB + 4;
    int* l_rf = l_rs + G1;

    const int tid0 = threadIdx.x;
    const int b = blockIdx.x;
    const int node0 = b * BS;
    const int nn = min(BS, N - node0);

    STAMP(0);
    int total = 0;
    for (int r0 = 0; r0 < G1; r0 += NTM) {
        int r = r0 + tid0;
        int s = 0, len = 0;
        if (r < G1) {
            s = off[(size_t)b * G1 + r];
            len = off[(size_t)(b + 1) * G1 + r] - s;
        }
        if (r0 == 0) {
            for (int i = tid0; i < 8 * 64; i += NTM) l_wo[i] = wof[i];
        }
        const int ln = tid0 & 63, wv = tid0 >> 6;
        int inc = len;
#pragma unroll
        for (int o = 1; o < 64; o <<= 1) {
            int t = __shfl_up(inc, o, 64);
            if (ln >= o) inc += t;
        }
        if (r0 > 0) __syncthreads();
        if (ln == 63) l_wsum[wv] = inc;
        __syncthreads();
        int pre = 0, tt = 0;
#pragma unroll
        for (int w = 0; w < NWM; ++w) {
            int v = l_wsum[w];
            pre += (w < wv) ? v : 0;
            tt += v;
        }
        if (r < G1) {
            l_rs[r] = s;
            l_rf[r] = total + pre + inc - len;
        }
        total += tt;
    }
    if (tid0 == 0) l_rf[G1] = total;
    STAMP(1);
    const int nchunks = max(1, (total + CAP - 1) / CAP);
    const __amdgpu_buffer_rsrc_t xrsrc =
        __builtin_amdgcn_make_buffer_rsrc((void*)xp, (short)0, (int)((uint32_t)N * 512u), 0x00020000);

#pragma unroll 1
    for (int ck = 0; ck < nchunks; ++ck) {
        const int c0 = ck * CAP;
        const int c1 = min(total, c0 + CAP);
        const int cn = c1 - c0;
        int tid = tid0;
        asm volatile("" : "+v"(tid));
        __syncthreads();
        for (int r = tid >> 3; r < G1; r += NTM / 8) {
            int f0 = l_rf[r], f1 = l_rf[r + 1];
            int base = r * CH + l_rs[r] - f0;
            int lo = max(f0, c0), hi = min(f1, c1);
            for (int f = lo + (tid & 7); f < hi; f += 8) {
                l_map[f - c0] = base + f;
                l_base[f - c0] = (uint32_t)r;
            }
        }
        const int ncell = nn * NKB;
        for (int i = tid; i < ncell; i += NTM) l_cell[i] = 0;
        if (tid == 0) *l_ctr = 0;
        __syncthreads();
        STAMP(2);
        uint2 ra[RPT], rb[RPT], rc[RPT];
        uint32_t key[RPT];
        int cell[RPT];
#pragma unroll
        for (int i = 0; i < RPT; ++i) {
            int f = tid + i * NTM;
            ra[i] = make_uint2(0u, 0u);
            rb[i] = make_uint2(0u, 0u);
            rc[i] = make_uint2(0u, 0u);
            key[i] = 0;
            if (f < cn) {
                int gi = l_map[f];
                const uint2* p = reinterpret_cast<const uint2*>(recs + (size_t)gi * 6);
                ra[i] = p[0];
                rb[i] = p[1];
                rc[i] = p[2];
                key[i] = l_base[f];
            }
        }
#pragma unroll
        for (int i = 0; i < RPT; ++i) {
            int f = tid + i * NTM;
            cell[i] = (int)(ra[i].y & 0xffffu) * NKB + (int)(key[i] >> kshift);
            key[i] = key[i] * (uint32_t)CH + (ra[i].y >> 16);
            if (f < cn) atomicAdd(&l_cell[cell[i]], 1);
        }
        __syncthreads();
        STAMP(3);
        {
            int cv[CPT];
            int sum = 0;
#pragma unroll
            for (int k = 0; k < CPT; ++k) {
                int idx = tid * CPT + k;
                cv[k] = (idx < ncell) ? l_cell[idx] : 0;
                sum += cv[k];
            }
            const int ln = tid & 63, wv = tid >> 6;
            int inc = sum;
#pragma unroll
            for (int o = 1; o < 64; o <<= 1) {
                int t = __shfl_up(inc, o, 64);
                if (ln >= o) inc += t;
            }
            if (ln == 63) l_wsum[wv] = inc;
            __syncthreads();
            int pre = inc - sum;
#pragma unroll
            for (int w = 0; w < NWM; ++w) pre += (w < wv) ? l_wsum[w] : 0;
#pragma unroll
            for (int k = 0; k < CPT; ++k) {
                int idx = tid * CPT + k;
                if (idx < ncell) {
                    l_cell[idx] = pre;
                    if ((idx & (NKB - 1)) == 0) l_start[idx / NKB] = pre;
                }
                pre += cv[k];
            }
            if (tid == 0) l_start[nn] = cn;
        }
        __syncthreads();
#pragma unroll
        for (int i = 0; i < RPT; ++i) {
            int f = tid + i * NTM;
            if (f < cn) {
                int pos = atomicAdd(&l_cell[cell[i]], 1);
                l_key[pos] = key[i];
            }
        }
        __syncthreads();
        STAMP(4);
#pragma unroll
        for (int i = 0; i < RPT; ++i) {
            int f = tid + i * NTM;
            if (f < cn) {
                int s0 = (cell[i] > 0) ? l_cell[cell[i] - 1] : 0;
                int s1 = l_cell[cell[i]];
                int rank = 0;
                for (int k = s0; k < s1; ++k) rank += (l_key[k] < key[i]) ? 1 : 0;
                int pos = s0 + rank;
                l_src[pos] = ra[i].x;
                l_sh[pos] = make_float4(bf_lo(rb[i].x), bf_hi(rb[i].x), bf_lo(rb[i].y), bf_hi(rb[i].y));
                l_ea[pos] = rc[i];
            }
        }
        __syncthreads();
        STAMP(5);
        if (nchunks == 1)
            node_loop<false>(smem, xrsrc, cg, w_r, out, accws, node0, nn, tid, true, true);
        else
            node_loop<true>(smem, xrsrc, cg, w_r, out, accws, node0, nn, tid, ck == 0, ck == nchunks - 1);
    }
}

extern "C" void kernel_launch(void* const* d_in, const int* in_sizes, int n_in,
                              void* d_out, int out_size, void* d_ws, size_t ws_size,
                              hipStream_t stream) {
    const uint4* x = (const uint4*)d_in[0];
    const int* src = (const int*)d_in[1];
    const int* dst = (const int*)d_in[2];
    const uint2* sh = (const uint2*)d_in[3];
    const uint2* ea = (const uint2*)d_in[4];
    const __hip_bfloat16* cg = (const __hip_bfloat16*)d_in[5];
    const __hip_bfloat16* w_r = (const __hip_bfloat16*)d_in[6];
    const __hip_bfloat16* w_out = (const __hip_bfloat16*)d_in[7];
    __hip_bfloat16* out = (__hip_bfloat16*)d_out;

    const int N = in_sizes[0] / 256;
    const int E = in_sizes[1];

    int G1 = 256;
    while ((long long)G1 * (NTP * EPT) < (long long)E) G1 += 256;
    int CH = (E + G1 - 1) / G1;
    if (CH < 1) CH = 1;
    int BS = (N + TGTNBK - 1) / TGTNBK;
    if (BS < 1) BS = 1;
    if (BS > MAXBS) BS = MAXBS;
    int NBK = (N + BS - 1) / BS;
    if (NBK < 1) NBK = 1;

    unsigned char* ws = (unsigned char*)d_ws;
    size_t xp_bytes = 0;
    uint32_t* recs = (uint32_t*)(ws + xp_bytes);
    size_t recs_bytes = ((size_t)G1 * CH * 24 + 255) & ~(size_t)255;
    int* off = (int*)(ws + xp_bytes + recs_bytes);
    size_t off_bytes = (((size_t)(NBK + 1) * G1 * 4) + 255) & ~(size_t)255;
    uint4* wof = (uint4*)(ws + xp_bytes + recs_bytes + off_bytes);
    float* accws = (float*)(ws + xp_bytes + recs_bytes + off_bytes + 8192);

    size_t lds_p = (size_t)MAXNBK * 4 + 128 + (size_t)CH * 24;
    if (lds_p < 16384) lds_p = 16384;
    (void)hipFuncSetAttribute((const void*)k_prep, hipFuncAttributeMaxDynamicSharedMemorySize, (int)lds_p);
    size_t lds_m = (size_t)LDS_CELL + (size_t)(BS * NKB + 4) * 4 + (size_t)(2 * G1 + 1) * 4;
    (void)hipFuncSetAttribute((const void*)k_main, hipFuncAttributeMaxDynamicSharedMemorySize, (int)lds_m);

    k_prep<<<G1, NTP, lds_p, stream>>>(src, dst, sh, ea, w_out, wof, E, CH, G1, BS, NBK, recs, off);
    int kshift = 0;
    while (((G1 - 1) >> kshift) >= NKB) ++kshift;
    k_main<<<NBK, NTM, lds_m, stream>>>(x, recs, off, cg, w_r, wof, out, accws, N, CH, G1, BS, kshift);
}
```

```cpp
#include <hip/hip_runtime.h>
#include <hip/hip_bf16.h>
#include <stdint.h>

typedef __attribute__((ext_vector_type(8))) __bf16 bf16x8_t;
typedef __attribute__((ext_vector_type(4))) float f32x4_t;
typedef __attribute__((ext_vector_type(4))) short v4i16_t;
typedef __attribute__((ext_vector_type(4))) unsigned int u32x4_t;
typedef __attribute__((ext_vector_type(2))) unsigned int u32x2_t;

#define NTP 1024
#define EPT 4
#define NTM 1024
#define NWM (NTM / 64)
#define CAP 3520
#define RPT ((CAP + NTM - 1) / NTM)
#define MAXBS 256
#define MAXNBK 1024
#define TGTNBK 256
#define STG_ROW 72

__device__ __forceinline__ float bf_lo(uint32_t v) { return __uint_as_float(v << 16); }
__device__ __forceinline__ float bf_hi(uint32_t v) { return __uint_as_float(v & 0xffff0000u); }

typedef __attribute__((ext_vector_type(2))) float f32x2_t;
typedef __attribute__((ext_vector_type(2))) __bf16 bf16x2_t;
__device__ __forceinline__ uint32_t pack_bf16(float a, float b) {
    f32x2_t v = {a, b};
    bf16x2_t r = __builtin_convertvector(v, bf16x2_t);
    return __builtin_bit_cast(uint32_t, r);
}

template <int NT>
__device__ __forceinline__ int block_excl_scan(int v, int* wsum, int* total) {
    const int lane = threadIdx.x & 63, wid = threadIdx.x >> 6;
    int inc = v;
#pragma unroll
    for (int o = 1; o < 64; o <<= 1) {
        int t = __shfl_up(inc, o, 64);
        if (lane >= o) inc += t;
    }
    __syncthreads();
    if (lane == 63) wsum[wid] = inc;
    __syncthreads();
    if (wid == 0) {
        int w = (lane < NT / 64) ? wsum[lane] : 0;
        int winc = w;
#pragma unroll
        for (int o = 1; o < 64; o <<= 1) {
            int t = __shfl_up(winc, o, 64);
            if (lane >= o) winc += t;
        }
        if (lane < NT / 64) wsum[lane] = winc - w;
        if (lane == NT / 64 - 1) wsum[NT / 64] = winc;
    }
    __syncthreads();
    int res = wsum[wid] + inc - v;
    *total = wsum[NT / 64];
    return res;
}

__global__ __launch_bounds__(NTP) void k_prep(
    const int* __restrict__ src, const int* __restrict__ dst,
    const uint2* __restrict__ sh, const uint2* __restrict__ ea,
    const __hip_bfloat16* __restrict__ w_out, uint4* __restrict__ wof,
    const uint4* __restrict__ x, size_t xvec,
    int E, int CH, int G1, int BS, int NBK,
    uint32_t* __restrict__ recs, int* __restrict__ off) {
    extern __shared__ __attribute__((aligned(16))) unsigned char smem[];
    const int tid = threadIdx.x;
    if (blockIdx.x == 0 && tid < 512) {
        const uint16_t* wo = reinterpret_cast<const uint16_t*>(w_out);
        int tk = tid >> 6, l = tid & 63;
        int t = tk >> 1, ks = tk & 1;
        int d = ((l & 15) >> 2) * 16 + 4 * t + (l & 3);
        int c0 = 32 * ks + 8 * (l >> 4);
        uint32_t v[4];
#pragma unroll
        for (int j = 0; j < 4; ++j)
            v[j] = (uint32_t)wo[(c0 + 2 * j) * 64 + d] | ((uint32_t)wo[(c0 + 2 * j + 1) * 64 + d] << 16);
        wof[tid] = make_uint4(v[0], v[1], v[2], v[3]);
    }
    if ((int)blockIdx.x >= G1) {
        const int pb = blockIdx.x - G1, npb = gridDim.x - G1;
        uint32_t a = 0;
        for (size_t i = (size_t)pb * NTP + tid; i < xvec; i += (size_t)npb * NTP * 4) {
            uint4 v0 = x[i], v1 = make_uint4(0u, 0u, 0u, 0u), v2 = v1, v3 = v1;
            const size_t st = (size_t)npb * NTP;
            if (i + st < xvec) v1 = x[i + st];
            if (i + 2 * st < xvec) v2 = x[i + 2 * st];
            if (i + 3 * st < xvec) v3 = x[i + 3 * st];
            a ^= v0.x ^ v1.y ^ v2.z ^ v3.w;
        }
        asm volatile("" ::"v"(a));
        return;
    }
    const int rid = blockIdx.x;

    int* hist = reinterpret_cast<int*>(smem);
    int* wsum = hist + MAXNBK;
    uint32_t* stage = reinterpret_cast<uint32_t*>(smem + MAXNBK * 4 + 128);
    const int blk = rid;
    const int e0 = blk * CH;
    const int e1 = min(E, e0 + CH);
    const int cnt = max(0, e1 - e0);

    int dv[EPT], sv[EPT];
    uint2 shv[EPT], eav[EPT];
#pragma unroll
    for (int i = 0; i < EPT; ++i) {
        int j = tid + i * NTP;
        dv[i] = 0; sv[i] = 0;
        shv[i] = make_uint2(0u, 0u);
        eav[i] = make_uint2(0u, 0u);
        if (j < cnt) {
            dv[i] = dst[e0 + j];
            sv[i] = src[e0 + j];
            shv[i] = sh[e0 + j];
            eav[i] = ea[e0 + j];
        }
    }
    for (int i = tid; i < NBK; i += NTP) hist[i] = 0;
    __syncthreads();

    int bk[EPT], dl[EPT];
#pragma unroll
    for (int i = 0; i < EPT; ++i) {
        int j = tid + i * NTP;
        int b = dv[i] / BS;
        bk[i] = b;
        dl[i] = dv[i] - b * BS;
        if (j < cnt) atomicAdd(&hist[b], 1);
    }
    __syncthreads();
    int hv = (tid < NBK) ? hist[tid] : 0;
    int total;
    int st = block_excl_scan<NTP>(hv, wsum, &total);
    if (tid < NBK) {
        hist[tid] = st;
        off[(size_t)tid * G1 + blk] = st;
    }
    if (tid == 0) off[(size_t)NBK * G1 + blk] = total;
    __syncthreads();
#pragma unroll
    for (int i = 0; i < EPT; ++i) {
        int j = tid + i * NTP;
        if (j < cnt) {
            int pos = atomicAdd(&hist[bk[i]], 1);
            uint2* p = reinterpret_cast<uint2*>(stage + (size_t)pos * 6);
            p[0] = make_uint2((uint32_t)sv[i], ((uint32_t)j << 16) | (uint32_t)dl[i]);
            p[1] = shv[i];
            p[2] = eav[i];
        }
    }
    __syncthreads();
    uint2* gout = reinterpret_cast<uint2*>(recs + (size_t)blk * CH * 6);
    const uint2* lin = reinterpret_cast<const uint2*>(stage);
    for (int i = tid; i < cnt * 3; i += NTP) gout[i] = lin[i];
}

#define STAMP(i) do {} while (0)
struct Cur {
    int nl, j0, je;
};

#define NKB 16
#define CPT ((MAXBS * NKB + NTM - 1) / NTM)
#define LDS_SH 0
#define LDS_EA (LDS_SH + (CAP + 8) * 16)
#define LDS_SRC (LDS_EA + (CAP + 8) * 8)
#define LDS_WO (LDS_SRC + (CAP + 8) * 4)
#define LDS_KEY (LDS_WO + 8192)
#define LDS_MAP LDS_SH
#define LDS_BASE (LDS_SH + CAP * 4)
#define LDS_STG LDS_KEY
#define LDS_START (LDS_KEY + CAP * 4)
#define LDS_WSUM (LDS_START + (MAXBS + 4) * 4)
#define LDS_CELL (LDS_WSUM + 32 * 4)

struct Frag {
    u32x2_t r[4];
};

template <bool MULTI>
__device__ __forceinline__ void node_loop(
    unsigned char* smem, const __amdgpu_buffer_rsrc_t xrsrc,
    const __hip_bfloat16* __restrict__ cg, const __hip_bfloat16* __restrict__ w_r,
    __hip_bfloat16* __restrict__ out, float* __restrict__ accws,
    int node0, int nn, int tid, bool first, bool last) {
    const float4* l_sh = reinterpret_cast<const float4*>(smem + LDS_SH);
    const uint16_t* l_ea = reinterpret_cast<const uint16_t*>(smem + LDS_EA);
    const uint32_t* l_src = reinterpret_cast<const uint32_t*>(smem + LDS_SRC);
    const uint4* l_wo = reinterpret_cast<const uint4*>(smem + LDS_WO);
    const int* l_start = reinterpret_cast<const int*>(smem + LDS_START);
    int* l_ctr = reinterpret_cast<int*>(smem + LDS_WSUM) + 24;
    const int lane = tid & 63, wid = __builtin_amdgcn_readfirstlane(tid >> 6);

    const int gq = lane >> 4;
    const int cl = lane & 15;
    float cgw[4][4];
#pragma unroll
    for (int u = 0; u < 4; ++u)
#pragma unroll
        for (int v = 0; v < 4; ++v) cgw[u][v] = __bfloat162float(cg[(u * 4 + v) * 4 + (lane & 3)]);
    float wrl[4];
#pragma unroll
    for (int t = 0; t < 4; ++t) wrl[t] = __bfloat162float(w_r[gq * 64 + 4 * cl + t]);
    const int cch = 4 * cl + gq;
    const int dsel = cl >> 2;
    uint16_t* stg = reinterpret_cast<uint16_t*>(smem + LDS_STG) + wid * 4 * STG_ROW;

    auto next_node = [&](Cur& c) {
        int v = 0;
        if (lane == 0) v = atomicAdd(l_ctr, 1);
        v = __builtin_amdgcn_readfirstlane(v);
        c.nl = v;
        c.j0 = 0;
        c.je = 0;
        if (v < nn) {
            c.j0 = __builtin_amdgcn_readfirstlane(l_start[v]);
            c.je = __builtin_amdgcn_readfirstlane(l_start[v + 1]);
        }
    };
    auto advance = [&](Cur& c) {
        if (c.nl < nn) {
            c.j0 += 4;
            if (c.j0 >= c.je) next_node(c);
        }
    };
    auto issue = [&](Frag& h, const Cur& c) {
        const int j = c.j0 + gq;
        const uint32_t s = l_src[j];
        uint32_t vo = (j < c.je) ? (s * 512u + (uint32_t)cl * 8u) : 0xFFFFF000u;
#pragma unroll
        for (int u = 0; u < 4; ++u) h.r[u] = __builtin_amdgcn_raw_buffer_load_b64(xrsrc, vo + u * 128, 0, 0);
    };
    f32x4_t acc[4];
#pragma unroll
    for (int t = 0; t < 4; ++t) acc[t] = (f32x4_t){0.f, 0.f, 0.f, 0.f};

    auto process = [&](const Frag& h, const Cur& c) {
        if (c.je > c.j0) {
        const int jr = c.j0 + gq;
        const int j = min(jr, c.je - 1);
        const float4 sv = l_sh[j];
        uint32_t edb = l_ea[j * 4 + dsel];
        const float ed = (jr < c.je) ? __uint_as_float(edb << 16) : 0.f;
        float p[4];
#pragma unroll
        for (int u = 0; u < 4; ++u) {
            float a = cgw[u][0] * sv.x;
            a = fmaf(cgw[u][1], sv.y, a);
            a = fmaf(cgw[u][2], sv.z, a);
            a = fmaf(cgw[u][3], sv.w, a);
            p[u] = a * ed;
        }
        uint32_t phi[2], plo[2];
#pragma unroll
        for (int k = 0; k < 2; ++k) {
            uint32_t hi = pack_bf16(p[2 * k], p[2 * k + 1]);
            float r0 = p[2 * k] - bf_lo(hi);
            float r1 = p[2 * k + 1] - bf_hi(hi);
            phi[k] = hi;
            plo[k] = pack_bf16(r0, r1);
        }
        const v4i16_t ahi = __builtin_bit_cast(v4i16_t, (u32x2_t){phi[0], phi[1]});
        const v4i16_t alo = __builtin_bit_cast(v4i16_t, (u32x2_t){plo[0], plo[1]});
        const v4i16_t b0 = __builtin_bit_cast(v4i16_t, (u32x2_t){__builtin_amdgcn_perm(h.r[1][0], h.r[0][0], 0x05040100u),
                                                                 __builtin_amdgcn_perm(h.r[3][0], h.r[2][0], 0x05040100u)});
        const v4i16_t b1 = __builtin_bit_cast(v4i16_t, (u32x2_t){__builtin_amdgcn_perm(h.r[1][0], h.r[0][0], 0x07060302u),
                                                                 __builtin_amdgcn_perm(h.r[3][0], h.r[2][0], 0x07060302u)});
        const v4i16_t b2 = __builtin_bit_cast(v4i16_t, (u32x2_t){__builtin_amdgcn_perm(h.r[1][1], h.r[0][1], 0x05040100u),
                                                                 __builtin_amdgcn_perm(h.r[3][1], h.r[2][1], 0x05040100u)});
        const v4i16_t b3 = __builtin_bit_cast(v4i16_t, (u32x2_t){__builtin_amdgcn_perm(h.r[1][1], h.r[0][1], 0x07060302u),
                                                                 __builtin_amdgcn_perm(h.r[3][1], h.r[2][1], 0x07060302u)});
        acc[0] = __builtin_amdgcn_mfma_f32_16x16x16bf16_1k(ahi, b0, acc[0], 0, 0, 0);
        acc[1] = __builtin_amdgcn_mfma_f32_16x16x16bf16_1k(ahi, b1, acc[1], 0, 0, 0);
        acc[2] = __builtin_amdgcn_mfma_f32_16x16x16bf16_1k(ahi, b2, acc[2], 0, 0, 0);
        acc[3] = __builtin_amdgcn_mfma_f32_16x16x16bf16_1k(ahi, b3, acc[3], 0, 0, 0);
        acc[0] = __builtin_amdgcn_mfma_f32_16x16x16bf16_1k(alo, b0, acc[0], 0, 0, 0);
        acc[1] = __builtin_amdgcn_mfma_f32_16x16x16bf16_1k(alo, b1, acc[1], 0, 0, 0);
        acc[2] = __builtin_amdgcn_mfma_f32_16x16x16bf16_1k(alo, b2, acc[2], 0, 0, 0);
        acc[3] = __builtin_amdgcn_mfma_f32_16x16x16bf16_1k(alo, b3, acc[3], 0, 0, 0);
        }
        if (c.j0 + 4 >= c.je) {
            float v[4][4];
#pragma unroll
            for (int t = 0; t < 4; ++t)
#pragma unroll
                for (int w = 0; w < 4; ++w) {
                    v[t][w] = acc[t][w] * wrl[t];
                    acc[t][w] = 0.f;
                }
            float r1[2][4];
#pragma unroll
            for (int t = 0; t < 2; ++t)
#pragma unroll
                for (int w = 0; w < 4; ++w) {
                    auto r = __builtin_amdgcn_permlane32_swap(__float_as_uint(v[t][w]), __float_as_uint(v[t + 2][w]),
                                                              false, false);
                    r1[t][w] = __uint_as_float(r[0]) + __uint_as_float(r[1]);
                }
            float ag[4];
#pragma unroll
            for (int w = 0; w < 4; ++w) {
                auto r = __builtin_amdgcn_permlane16_swap(__float_as_uint(r1[0][w]), __float_as_uint(r1[1][w]),
                                                          false, false);
                ag[w] = __uint_as_float(r[0]) + __uint_as_float(r[1]);
            }
            if (MULTI) {
                float* ap = accws + (size_t)(node0 + c.nl) * 256 + cch;
                if (!first) {
#pragma unroll
                    for (int w = 0; w < 4; ++w) ag[w] += ap[w * 64];
                }
                if (!last) {
#pragma unroll
                    for (int w = 0; w < 4; ++w) ap[w * 64] = ag[w];
                }
            }
            if (!MULTI || last) {
                uint32_t p01 = pack_bf16(ag[0], ag[1]);
                uint32_t p23 = pack_bf16(ag[2], ag[3]);
                stg[0 * STG_ROW + cch] = (uint16_t)(p01 & 0xffffu);
                stg[1 * STG_ROW + cch] = (uint16_t)(p01 >> 16);
                stg[2 * STG_ROW + cch] = (uint16_t)(p23 & 0xffffu);
                stg[3 * STG_ROW + cch] = (uint16_t)(p23 >> 16);
                __builtin_amdgcn_wave_barrier();
                bf16x8_t bfr[2];
#pragma unroll
                for (int ks = 0; ks < 2; ++ks) {
                    uint4 vv = *reinterpret_cast<const uint4*>(stg + (lane & 3) * STG_ROW + 32 * ks + 8 * gq);
                    bfr[ks] = __builtin_bit_cast(bf16x8_t, vv);
                }
                uint32_t ow[8];
#pragma unroll
                for (int t = 0; t < 4; ++t) {
                    f32x4_t d4 = {0.f, 0.f, 0.f, 0.f};
#pragma unroll
                    for (int ks = 0; ks < 2; ++ks) {
                        bf16x8_t afr = __builtin_bit_cast(bf16x8_t, l_wo[(t * 2 + ks) * 64 + lane]);
                        d4 = __builtin_amdgcn_mfma_f32_16x16x32_bf16(afr, bfr[ks], d4, 0, 0, 0);
                    }
                    ow[2 * t] = pack_bf16(d4[0], d4[1]);
                    ow[2 * t + 1] = pack_bf16(d4[2], d4[3]);
                }
                __builtin_amdgcn_wave_barrier();
                if (cl < 4) {
                    uint4* op = reinterpret_cast<uint4*>(out + ((size_t)(node0 + c.nl) * 4 + cl) * 64 + gq * 16);
                    op[0] = make_uint4(ow[0], ow[1], ow[2], ow[3]);
                    op[1] = make_uint4(ow[4], ow[5], ow[6], ow[7]);
                }
            }
        }
    };

    Frag h0, h1, h2, h3;
    Cur c0, c1, c2, c3;
    next_node(c0);
    issue(h0, c0);
    c1 = c0; advance(c1); issue(h1, c1);
    c2 = c1; advance(c2); issue(h2, c2);
    c3 = c2; advance(c3); issue(h3, c3);
#pragma unroll 1
    while (c0.nl < nn) {
        process(h0, c0); c0 = c3; advance(c0); issue(h0, c0);
        if (c1.nl < nn) process(h1, c1);
        c1 = c0; advance(c1); issue(h1, c1);
        if (c2.nl < nn) process(h2, c2);
        c2 = c1; advance(c2); issue(h2, c2);
        if (c3.nl < nn) process(h3, c3);
        c3 = c2; advance(c3); issue(h3, c3);
    }
}

__global__ __launch_bounds__(NTM) void k_main(
    const uint4* __restrict__ xp, const uint32_t* __restrict__ recs, const int* __restrict__ off,
    const __hip_bfloat16* __restrict__ cg, const __hip_bfloat16* __restrict__ w_r,
    const uint4* __restrict__ wof, __hip_bfloat16* __restrict__ out,
    float* __restrict__ accws, int N, int CH, int G1, int BS, int kshift) {
    extern __shared__ __attribute__((aligned(16))) unsigned char smem[];
    float4* l_sh = reinterpret_cast<float4*>(smem + LDS_SH);
    uint2* l_ea = reinterpret_cast<uint2*>(smem + LDS_EA);
    uint32_t* l_src = reinterpret_cast<uint32_t*>(smem + LDS_SRC);
    uint4* l_wo = reinterpret_cast<uint4*>(smem + LDS_WO);
    int* l_map = reinterpret_cast<int*>(smem + LDS_MAP);
    uint32_t* l_key = reinterpret_cast<uint32_t*>(smem + LDS_KEY);
    uint32_t* l_base = reinterpret_cast<uint32_t*>(smem + LDS_BASE);
    int* l_start = reinterpret_cast<int*>(smem + LDS_START);
    int* l_wsum = reinterpret_cast<int*>(smem + LDS_WSUM);
    int* l_ctr = l_wsum + 24;
    int* l_cell = reinterpret_cast<int*>(smem + LDS_CELL);
    int* l_rs = l_cell + BS * NKB + 4;
    int* l_rf = l_rs + G1;

    const int tid0 = threadIdx.x;
    const int b = blockIdx.x;
    const int node0 = b * BS;
    const int nn = min(BS, N - node0);

    STAMP(0);
    int total = 0;
    for (int r0 = 0; r0 < G1; r0 += NTM) {
        int r = r0 + tid0;
        int s = 0, len = 0;
        if (r < G1) {
            s = off[(size_t)b * G1 + r];
            len = off[(size_t)(b + 1) * G1 + r] - s;
        }
        if (r0 == 0) {
            for (int i = tid0; i < 8 * 64; i += NTM) l_wo[i] = wof[i];
        }
        const int ln = tid0 & 63, wv = tid0 >> 6;
        int inc = len;
#pragma unroll
        for (int o = 1; o < 64; o <<= 1) {
            int t = __shfl_up(inc, o, 64);
            if (ln >= o) inc += t;
        }
        if (r0 > 0) __syncthreads();
        if (ln == 63) l_wsum[wv] = inc;
        __syncthreads();
        int pre = 0, tt = 0;
#pragma unroll
        for (int w = 0; w < NWM; ++w) {
            int v = l_wsum[w];
            pre += (w < wv) ? v : 0;
            tt += v;
        }
        if (r < G1) {
            l_rs[r] = s;
            l_rf[r] = total + pre + inc - len;
        }
        total += tt;
    }
    if (tid0 == 0) l_rf[G1] = total;
    STAMP(1);
    const int nchunks = max(1, (total + CAP - 1) / CAP);
    const __amdgpu_buffer_rsrc_t xrsrc =
        __builtin_amdgcn_make_buffer_rsrc((void*)xp, (short)0, (int)((uint32_t)N * 512u), 0x00020000);

#pragma unroll 1
    for (int ck = 0; ck < nchunks; ++ck) {
        const int c0 = ck * CAP;
        const int c1 = min(total, c0 + CAP);
        const int cn = c1 - c0;
        int tid = tid0;
        asm volatile("" : "+v"(tid));
        __syncthreads();
        for (int r = tid >> 3; r < G1; r += NTM / 8) {
            int f0 = l_rf[r], f1 = l_rf[r + 1];
            int base = r * CH + l_rs[r] - f0;
            int lo = max(f0, c0), hi = min(f1, c1);
            for (int f = lo + (tid & 7); f < hi; f += 8) {
                l_map[f - c0] = base + f;
                l_base[f - c0] = (uint32_t)r;
            }
        }
        const int ncell = nn * NKB;
        for (int i = tid; i < ncell; i += NTM) l_cell[i] = 0;
        if (tid == 0) *l_ctr = 0;
        __syncthreads();
        STAMP(2);
        uint2 ra[RPT], rb[RPT], rc[RPT];
        uint32_t key[RPT];
        int cell[RPT];
#pragma unroll
        for (int i = 0; i < RPT; ++i) {
            int f = tid + i * NTM;
            ra[i] = make_uint2(0u, 0u);
            rb[i] = make_uint2(0u, 0u);
            rc[i] = make_uint2(0u, 0u);
            key[i] = 0;
            if (f < cn) {
                int gi = l_map[f];
                const uint2* p = reinterpret_cast<const uint2*>(recs + (size_t)gi * 6);
                ra[i] = p[0];
                rb[i] = p[1];
                rc[i] = p[2];
                key[i] = l_base[f];
            }
        }
#pragma unroll
        for (int i = 0; i < RPT; ++i) {
            int f = tid + i * NTM;
            cell[i] = (int)(ra[i].y & 0xffffu) * NKB + (int)(key[i] >> kshift);
            key[i] = key[i] * (uint32_t)CH + (ra[i].y >> 16);
            if (f < cn) atomicAdd(&l_cell[cell[i]], 1);
        }
        __syncthreads();
        STAMP(3);
        {
            int cv[CPT];
            int sum = 0;
#pragma unroll
            for (int k = 0; k < CPT; ++k) {
                int idx = tid * CPT + k;
                cv[k] = (idx < ncell) ? l_cell[idx] : 0;
                sum += cv[k];
            }
            const int ln = tid & 63, wv = tid >> 6;
            int inc = sum;
#pragma unroll
            for (int o = 1; o < 64; o <<= 1) {
                int t = __shfl_up(inc, o, 64);
                if (ln >= o) inc += t;
            }
            if (ln == 63) l_wsum[wv] = inc;
            __syncthreads();
            int pre = inc - sum;
#pragma unroll
            for (int w = 0; w < NWM; ++w) pre += (w < wv) ? l_wsum[w] : 0;
#pragma unroll
            for (int k = 0; k < CPT; ++k) {
                int idx = tid * CPT + k;
                if (idx < ncell) {
                    l_cell[idx] = pre;
                    if ((idx & (NKB - 1)) == 0) l_start[idx / NKB] = pre;
                }
                pre += cv[k];
            }
            if (tid == 0) l_start[nn] = cn;
        }
        __syncthreads();
#pragma unroll
        for (int i = 0; i < RPT; ++i) {
            int f = tid + i * NTM;
            if (f < cn) {
                int pos = atomicAdd(&l_cell[cell[i]], 1);
                l_key[pos] = key[i];
            }
        }
        __syncthreads();
        STAMP(4);
#pragma unroll
        for (int i = 0; i < RPT; ++i) {
            int f = tid + i * NTM;
            if (f < cn) {
                int s0 = (cell[i] > 0) ? l_cell[cell[i] - 1] : 0;
                int s1 = l_cell[cell[i]];
                int rank = 0;
                for (int k = s0; k < s1; ++k) rank += (l_key[k] < key[i]) ? 1 : 0;
                int pos = s0 + rank;
                l_src[pos] = ra[i].x;
                l_sh[pos] = make_float4(bf_lo(rb[i].x), bf_hi(rb[i].x), bf_lo(rb[i].y), bf_hi(rb[i].y));
                l_ea[pos] = rc[i];
            }
        }
        __syncthreads();
        STAMP(5);
        if (nchunks == 1)
            node_loop<false>(smem, xrsrc, cg, w_r, out, accws, node0, nn, tid, true, true);
        else
            node_loop<true>(smem, xrsrc, cg, w_r, out, accws, node0, nn, tid, ck == 0, ck == nchunks - 1);
    }
}

extern "C" void kernel_launch(void* const* d_in, const int* in_sizes, int n_in,
                              void* d_out, int out_size, void* d_ws, size_t ws_size,
                              hipStream_t stream) {
    const uint4* x = (const uint4*)d_in[0];
    const int* src = (const int*)d_in[1];
    const int* dst = (const int*)d_in[2];
    const uint2* sh = (const uint2*)d_in[3];
    const uint2* ea = (const uint2*)d_in[4];
    const __hip_bfloat16* cg = (const __hip_bfloat16*)d_in[5];
    const __hip_bfloat16* w_r = (const __hip_bfloat16*)d_in[6];
    const __hip_bfloat16* w_out = (const __hip_bfloat16*)d_in[7];
    __hip_bfloat16* out = (__hip_bfloat16*)d_out;

    const int N = in_sizes[0] / 256;
    const int E = in_sizes[1];

    int G1 = 256;
    while ((long long)G1 * (NTP * EPT) < (long long)E) G1 += 256;
    int CH = (E + G1 - 1) / G1;
    if (CH < 1) CH = 1;
    int BS = (N + TGTNBK - 1) / TGTNBK;
    if (BS < 1) BS = 1;
    if (BS > MAXBS) BS = MAXBS;
    int NBK = (N + BS - 1) / BS;
    if (NBK < 1) NBK = 1;

    unsigned char* ws = (unsigned char*)d_ws;
    size_t xp_bytes = 0;
    uint32_t* recs = (uint32_t*)(ws + xp_bytes);
    size_t recs_bytes = ((size_t)G1 * CH * 24 + 255) & ~(size_t)255;
    int* off = (int*)(ws + xp_bytes + recs_bytes);
    size_t off_bytes = (((size_t)(NBK + 1) * G1 * 4) + 255) & ~(size_t)255;
    uint4* wof = (uint4*)(ws + xp_bytes + recs_bytes + off_bytes);
    float* accws = (float*)(ws + xp_bytes + recs_bytes + off_bytes + 8192);

    size_t lds_p = (size_t)MAXNBK * 4 + 128 + (size_t)CH * 24;
    if (lds_p < 16384) lds_p = 16384;
    (void)hipFuncSetAttribute((const void*)k_prep, hipFuncAttributeMaxDynamicSharedMemorySize, (int)lds_p);
    size_t lds_m = (size_t)LDS_CELL + (size_t)(BS * NKB + 4) * 4 + (size_t)(2 * G1 + 1) * 4;
    (void)hipFuncSetAttribute((const void*)k_main, hipFuncAttributeMaxDynamicSharedMemorySize, (int)lds_m);

    k_prep<<<G1 + 256, NTP, lds_p, stream>>>(src, dst, sh, ea, w_out, wof, x, (size_t)N * 32, E, CH, G1, BS, NBK, recs, off);
    int kshift = 0;
    while (((G1 - 1) >> kshift) >= NKB) ++kshift;
    k_main<<<NBK, NTM, lds_m, stream>>>(x, recs, off, cg, w_r, wof, out, accws, N, CH, G1, BS, kshift);
}
```

```cpp
#include <hip/hip_runtime.h>
#include <hip/hip_bf16.h>
#include <stdint.h>

typedef __attribute__((ext_vector_type(8))) __bf16 bf16x8_t;
typedef __attribute__((ext_vector_type(4))) float f32x4_t;
typedef __attribute__((ext_vector_type(4))) short v4i16_t;
typedef __attribute__((ext_vector_type(4))) unsigned int u32x4_t;
typedef __attribute__((ext_vector_type(2))) unsigned int u32x2_t;

#define NTP 1024
#define EPT 4
#define NTM 1024
#define NWM (NTM / 64)
#define CAP 3520
#define RPT ((CAP + NTM - 1) / NTM)
#define MAXBS 256
#define MAXNBK 1024
#define TGTNBK 256
#define STG_ROW 72

__device__ __forceinline__ float bf_lo(uint32_t v) { return __uint_as_float(v << 16); }
__device__ __forceinline__ float bf_hi(uint32_t v) { return __uint_as_float(v & 0xffff0000u); }

typedef __attribute__((ext_vector_type(2))) float f32x2_t;
typedef __attribute__((ext_vector_type(2))) __bf16 bf16x2_t;
__device__ __forceinline__ uint32_t pack_bf16(float a, float b) {
    f32x2_t v = {a, b};
    bf16x2_t r = __builtin_convertvector(v, bf16x2_t);
    return __builtin_bit_cast(uint32_t, r);
}

template <int NT>
__device__ __forceinline__ int block_excl_scan(int v, int* wsum, int* total) {
    const int lane = threadIdx.x & 63, wid = threadIdx.x >> 6;
    int inc = v;
#pragma unroll
    for (int o = 1; o < 64; o <<= 1) {
        int t = __shfl_up(inc, o, 64);
        if (lane >= o) inc += t;
    }
    __syncthreads();
    if (lane == 63) wsum[wid] = inc;
    __syncthreads();
    if (wid == 0) {
        int w = (lane < NT / 64) ? wsum[lane] : 0;
        int winc = w;
#pragma unroll
        for (int o = 1; o < 64; o <<= 1) {
            int t = __shfl_up(winc, o, 64);
            if (lane >= o) winc += t;
        }
        if (lane < NT / 64) wsum[lane] = winc - w;
        if (lane == NT / 64 - 1) wsum[NT / 64] = winc;
    }
    __syncthreads();
    int res = wsum[wid] + inc - v;
    *total = wsum[NT / 64];
    return res;
}

__global__ __launch_bounds__(NTP) void k_prep(
    const int* __restrict__ src, const int* __restrict__ dst,
    const uint2* __restrict__ sh, const uint2* __restrict__ ea,
    const __hip_bfloat16* __restrict__ w_out, uint4* __restrict__ wof,
    int E, int CH, int G1, int BS, int NBK,
    uint32_t* __restrict__ recs, int* __restrict__ off) {
    extern __shared__ __attribute__((aligned(16))) unsigned char smem[];
    const int tid = threadIdx.x;
    if (blockIdx.x == 0 && tid < 512) {
        const uint16_t* wo = reinterpret_cast<const uint16_t*>(w_out);
        int tk = tid >> 6, l = tid & 63;
        int t = tk >> 1, ks = tk & 1;
        int d = ((l & 15) >> 2) * 16 + 4 * t + (l & 3);
        int c0 = 32 * ks + 8 * (l >> 4);
        uint32_t v[4];
#pragma unroll
        for (int j = 0; j < 4; ++j)
            v[j] = (uint32_t)wo[(c0 + 2 * j) * 64 + d] | ((uint32_t)wo[(c0 + 2 * j + 1) * 64 + d] << 16);
        wof[tid] = make_uint4(v[0], v[1], v[2], v[3]);
    }
    const int rid = blockIdx.x;

    int* hist = reinterpret_cast<int*>(smem);
    int* wsum = hist + MAXNBK;
    uint32_t* stage = reinterpret_cast<uint32_t*>(smem + MAXNBK * 4 + 128);
    const int blk = rid;
    const int e0 = blk * CH;
    const int e1 = min(E, e0 + CH);
    const int cnt = max(0, e1 - e0);

    int dv[EPT], sv[EPT];
    uint2 shv[EPT], eav[EPT];
#pragma unroll
    for (int i = 0; i < EPT; ++i) {
        int j = tid + i * NTP;
        dv[i] = 0; sv[i] = 0;
        shv[i] = make_uint2(0u, 0u);
        eav[i] = make_uint2(0u, 0u);
        if (j < cnt) {
            dv[i] = dst[e0 + j];
            sv[i] = src[e0 + j];
            shv[i] = sh[e0 + j];
            eav[i] = ea[e0 + j];
        }
    }
    for (int i = tid; i < NBK; i += NTP) hist[i] = 0;
    __syncthreads();

    int bk[EPT], dl[EPT];
#pragma unroll
    for (int i = 0; i < EPT; ++i) {
        int j = tid + i * NTP;
        int b = dv[i] / BS;
        bk[i] = b;
        dl[i] = dv[i] - b * BS;
        if (j < cnt) atomicAdd(&hist[b], 1);
    }
    __syncthreads();
    int hv = (tid < NBK) ? hist[tid] : 0;
    int total;
    int st = block_excl_scan<NTP>(hv, wsum, &total);
    if (tid < NBK) {
        hist[tid] = st;
        off[(size_t)tid * G1 + blk] = st;
    }
    if (tid == 0) off[(size_t)NBK * G1 + blk] = total;
    __syncthreads();
#pragma unroll
    for (int i = 0; i < EPT; ++i) {
        int j = tid + i * NTP;
        if (j < cnt) {
            int pos = atomicAdd(&hist[bk[i]], 1);
            uint2* p = reinterpret_cast<uint2*>(stage + (size_t)pos * 6);
            p[0] = make_uint2((uint32_t)sv[i], ((uint32_t)j << 16) | (uint32_t)dl[i]);
            p[1] = shv[i];
            p[2] = eav[i];
        }
    }
    __syncthreads();
    uint2* gout = reinterpret_cast<uint2*>(recs + (size_t)blk * CH * 6);
    const uint2* lin = reinterpret_cast<const uint2*>(stage);
    {
        unsigned long long* g8 = reinterpret_cast<unsigned long long*>(gout);
        const unsigned long long* l8 = reinterpret_cast<const unsigned long long*>(lin);
        for (int i = tid; i < cnt * 3; i += NTP) __builtin_nontemporal_store(l8[i], g8 + i);
    }
}

#define STAMP(i) do {} while (0)
struct Cur {
    int nl, j0, je;
};

#define NKB 16
#define CPT ((MAXBS * NKB + NTM - 1) / NTM)
#define LDS_SH 0
#define LDS_EA (LDS_SH + (CAP + 8) * 16)
#define LDS_SRC (LDS_EA + (CAP + 8) * 8)
#define LDS_WO (LDS_SRC + (CAP + 8) * 4)
#define LDS_KEY (LDS_WO + 8192)
#define LDS_MAP LDS_SH
#define LDS_BASE (LDS_SH + CAP * 4)
#define LDS_STG LDS_KEY
#define LDS_START (LDS_KEY + CAP * 4)
#define LDS_WSUM (LDS_START + (MAXBS + 4) * 4)
#define LDS_CELL (LDS_WSUM + 32 * 4)

struct Frag {
    u32x2_t r[4];
};

template <bool MULTI>
__device__ __forceinline__ void node_loop(
    unsigned char* smem, const __amdgpu_buffer_rsrc_t xrsrc,
    const __hip_bfloat16* __restrict__ cg, const __hip_bfloat16* __restrict__ w_r,
    __hip_bfloat16* __restrict__ out, float* __restrict__ accws,
    int node0, int nn, int tid, bool first, bool last) {
    const float4* l_sh = reinterpret_cast<const float4*>(smem + LDS_SH);
    const uint16_t* l_ea = reinterpret_cast<const uint16_t*>(smem + LDS_EA);
    const uint32_t* l_src = reinterpret_cast<const uint32_t*>(smem + LDS_SRC);
    const uint4* l_wo = reinterpret_cast<const uint4*>(smem + LDS_WO);
    const int* l_start = reinterpret_cast<const int*>(smem + LDS_START);
    int* l_ctr = reinterpret_cast<int*>(smem + LDS_WSUM) + 24;
    const int lane = tid & 63, wid = __builtin_amdgcn_readfirstlane(tid >> 6);

    const int gq = lane >> 4;
    const int cl = lane & 15;
    float cgw[4][4];
#pragma unroll
    for (int u = 0; u < 4; ++u)
#pragma unroll
        for (int v = 0; v < 4; ++v) cgw[u][v] = __bfloat162float(cg[(u * 4 + v) * 4 + (lane & 3)]);
    float wrl[4];
#pragma unroll
    for (int t = 0; t < 4; ++t) wrl[t] = __bfloat162float(w_r[gq * 64 + 4 * cl + t]);
    const int cch = 4 * cl + gq;
    const int dsel = cl >> 2;
    uint16_t* stg = reinterpret_cast<uint16_t*>(smem + LDS_STG) + wid * 4 * STG_ROW;

    auto next_node = [&](Cur& c) {
        int v = 0;
        if (lane == 0) v = atomicAdd(l_ctr, 1);
        v = __builtin_amdgcn_readfirstlane(v);
        c.nl = v;
        c.j0 = 0;
        c.je = 0;
        if (v < nn) {
            c.j0 = __builtin_amdgcn_readfirstlane(l_start[v]);
            c.je = __builtin_amdgcn_readfirstlane(l_start[v + 1]);
        }
    };
    auto advance = [&](Cur& c) {
        if (c.nl < nn) {
            c.j0 += 4;
            if (c.j0 >= c.je) next_node(c);
        }
    };
    auto issue = [&](Frag& h, const Cur& c) {
        const int j = c.j0 + gq;
        const uint32_t s = l_src[j];
        uint32_t vo = (j < c.je) ? (s * 512u + (uint32_t)cl * 8u) : 0xFFFFF000u;
#pragma unroll
        for (int u = 0; u < 4; ++u) h.r[u] = __builtin_amdgcn_raw_buffer_load_b64(xrsrc, vo + u * 128, 0, 0);
    };
    f32x4_t acc[4];
#pragma unroll
    for (int t = 0; t < 4; ++t) acc[t] = (f32x4_t){0.f, 0.f, 0.f, 0.f};

    auto process = [&](const Frag& h, const Cur& c) {
        if (c.je > c.j0) {
        const int jr = c.j0 + gq;
        const int j = min(jr, c.je - 1);
        const float4 sv = l_sh[j];
        uint32_t edb = l_ea[j * 4 + dsel];
        const float ed = (jr < c.je) ? __uint_as_float(edb << 16) : 0.f;
        float p[4];
#pragma unroll
        for (int u = 0; u < 4; ++u) {
            float a = cgw[u][0] * sv.x;
            a = fmaf(cgw[u][1], sv.y, a);
            a = fmaf(cgw[u][2], sv.z, a);
            a = fmaf(cgw[u][3], sv.w, a);
            p[u] = a * ed;
        }
        uint32_t phi[2], plo[2];
#pragma unroll
        for (int k = 0; k < 2; ++k) {
            uint32_t hi = pack_bf16(p[2 * k], p[2 * k + 1]);
            float r0 = p[2 * k] - bf_lo(hi);
            float r1 = p[2 * k + 1] - bf_hi(hi);
            phi[k] = hi;
            plo[k] = pack_bf16(r0, r1);
        }
        const v4i16_t ahi = __builtin_bit_cast(v4i16_t, (u32x2_t){phi[0], phi[1]});
        const v4i16_t alo = __builtin_bit_cast(v4i16_t, (u32x2_t){plo[0], plo[1]});
        const v4i16_t b0 = __builtin_bit_cast(v4i16_t, (u32x2_t){__builtin_amdgcn_perm(h.r[1][0], h.r[0][0], 0x05040100u),
                                                                 __builtin_amdgcn_perm(h.r[3][0], h.r[2][0], 0x05040100u)});
        const v4i16_t b1 = __builtin_bit_cast(v4i16_t, (u32x2_t){__builtin_amdgcn_perm(h.r[1][0], h.r[0][0], 0x07060302u),
                                                                 __builtin_amdgcn_perm(h.r[3][0], h.r[2][0], 0x07060302u)});
        const v4i16_t b2 = __builtin_bit_cast(v4i16_t, (u32x2_t){__builtin_amdgcn_perm(h.r[1][1], h.r[0][1], 0x05040100u),
                                                                 __builtin_amdgcn_perm(h.r[3][1], h.r[2][1], 0x05040100u)});
        const v4i16_t b3 = __builtin_bit_cast(v4i16_t, (u32x2_t){__builtin_amdgcn_perm(h.r[1][1], h.r[0][1], 0x07060302u),
                                                                 __builtin_amdgcn_perm(h.r[3][1], h.r[2][1], 0x07060302u)});
        acc[0] = __builtin_amdgcn_mfma_f32_16x16x16bf16_1k(ahi, b0, acc[0], 0, 0, 0);
        acc[1] = __builtin_amdgcn_mfma_f32_16x16x16bf16_1k(ahi, b1, acc[1], 0, 0, 0);
        acc[2] = __builtin_amdgcn_mfma_f32_16x16x16bf16_1k(ahi, b2, acc[2], 0, 0, 0);
        acc[3] = __builtin_amdgcn_mfma_f32_16x16x16bf16_1k(ahi, b3, acc[3], 0, 0, 0);
        acc[0] = __builtin_amdgcn_mfma_f32_16x16x16bf16_1k(alo, b0, acc[0], 0, 0, 0);
        acc[1] = __builtin_amdgcn_mfma_f32_16x16x16bf16_1k(alo, b1, acc[1], 0, 0, 0);
        acc[2] = __builtin_amdgcn_mfma_f32_16x16x16bf16_1k(alo, b2, acc[2], 0, 0, 0);
        acc[3] = __builtin_amdgcn_mfma_f32_16x16x16bf16_1k(alo, b3, acc[3], 0, 0, 0);
        }
        if (c.j0 + 4 >= c.je) {
            float v[4][4];
#pragma unroll
            for (int t = 0; t < 4; ++t)
#pragma unroll
                for (int w = 0; w < 4; ++w) {
                    v[t][w] = acc[t][w] * wrl[t];
                    acc[t][w] = 0.f;
                }
            float r1[2][4];
#pragma unroll
            for (int t = 0; t < 2; ++t)
#pragma unroll
                for (int w = 0; w < 4; ++w) {
                    auto r = __builtin_amdgcn_permlane32_swap(__float_as_uint(v[t][w]), __float_as_uint(v[t + 2][w]),
                                                              false, false);
                    r1[t][w] = __uint_as_float(r[0]) + __uint_as_float(r[1]);
                }
            float ag[4];
#pragma unroll
            for (int w = 0; w < 4; ++w) {
                auto r = __builtin_amdgcn_permlane16_swap(__float_as_uint(r1[0][w]), __float_as_uint(r1[1][w]),
                                                          false, false);
                ag[w] = __uint_as_float(r[0]) + __uint_as_float(r[1]);
            }
            if (MULTI) {
                float* ap = accws + (size_t)(node0 + c.nl) * 256 + cch;
                if (!first) {
#pragma unroll
                    for (int w = 0; w < 4; ++w) ag[w] += ap[w * 64];
                }
                if (!last) {
#pragma unroll
                    for (int w = 0; w < 4; ++w) ap[w * 64] = ag[w];
                }
            }
            if (!MULTI || last) {
                uint32_t p01 = pack_bf16(ag[0], ag[1]);
                uint32_t p23 = pack_bf16(ag[2], ag[3]);
                stg[0 * STG_ROW + cch] = (uint16_t)(p01 & 0xffffu);
                stg[1 * STG_ROW + cch] = (uint16_t)(p01 >> 16);
                stg[2 * STG_ROW + cch] = (uint16_t)(p23 & 0xffffu);
                stg[3 * STG_ROW + cch] = (uint16_t)(p23 >> 16);
                __builtin_amdgcn_wave_barrier();
                bf16x8_t bfr[2];
#pragma unroll
                for (int ks = 0; ks < 2; ++ks) {
                    uint4 vv = *reinterpret_cast<const uint4*>(stg + (lane & 3) * STG_ROW + 32 * ks + 8 * gq);
                    bfr[ks] = __builtin_bit_cast(bf16x8_t, vv);
                }
                uint32_t ow[8];
#pragma unroll
                for (int t = 0; t < 4; ++t) {
                    f32x4_t d4 = {0.f, 0.f, 0.f, 0.f};
#pragma unroll
                    for (int ks = 0; ks < 2; ++ks) {
                        bf16x8_t afr = __builtin_bit_cast(bf16x8_t, l_wo[(t * 2 + ks) * 64 + lane]);
                        d4 = __builtin_amdgcn_mfma_f32_16x16x32_bf16(afr, bfr[ks], d4, 0, 0, 0);
                    }
                    ow[2 * t] = pack_bf16(d4[0], d4[1]);
                    ow[2 * t + 1] = pack_bf16(d4[2], d4[3]);
                }
                __builtin_amdgcn_wave_barrier();
                if (cl < 4) {
                    uint4* op = reinterpret_cast<uint4*>(out + ((size_t)(node0 + c.nl) * 4 + cl) * 64 + gq * 16);
                    op[0] = make_uint4(ow[0], ow[1], ow[2], ow[3]);
                    op[1] = make_uint4(ow[4], ow[5], ow[6], ow[7]);
                }
            }
        }
    };

    Frag h0, h1, h2;
    Cur c0, c1, c2;
    next_node(c0);
    issue(h0, c0);
    c1 = c0; advance(c1); issue(h1, c1);
    c2 = c1; advance(c2); issue(h2, c2);
#pragma unroll 1
    while (c0.nl < nn) {
        process(h0, c0); c0 = c2; advance(c0); issue(h0, c0);
        if (c1.nl < nn) process(h1, c1);
        c1 = c0; advance(c1); issue(h1, c1);
        if (c2.nl < nn) process(h2, c2);
        c2 = c1; advance(c2); issue(h2, c2);
    }
}

__global__ __launch_bounds__(NTM) void k_main(
    const uint4* __restrict__ xp, const uint32_t* __restrict__ recs, const int* __restrict__ off,
    const __hip_bfloat16* __restrict__ cg, const __hip_bfloat16* __restrict__ w_r,
    const uint4* __restrict__ wof, __hip_bfloat16* __restrict__ out,
    float* __restrict__ accws, int N, int CH, int G1, int BS, int kshift) {
    extern __shared__ __attribute__((aligned(16))) unsigned char smem[];
    float4* l_sh = reinterpret_cast<float4*>(smem + LDS_SH);
    uint2* l_ea = reinterpret_cast<uint2*>(smem + LDS_EA);
    uint32_t* l_src = reinterpret_cast<uint32_t*>(smem + LDS_SRC);
    uint4* l_wo = reinterpret_cast<uint4*>(smem + LDS_WO);
    int* l_map = reinterpret_cast<int*>(smem + LDS_MAP);
    uint32_t* l_key = reinterpret_cast<uint32_t*>(smem + LDS_KEY);
    uint32_t* l_base = reinterpret_cast<uint32_t*>(smem + LDS_BASE);
    int* l_start = reinterpret_cast<int*>(smem + LDS_START);
    int* l_wsum = reinterpret_cast<int*>(smem + LDS_WSUM);
    int* l_ctr = l_wsum + 24;
    int* l_cell = reinterpret_cast<int*>(smem + LDS_CELL);
    int* l_rs = l_cell + BS * NKB + 4;
    int* l_rf = l_rs + G1;

    const int tid0 = threadIdx.x;
    const int b = blockIdx.x;
    const int node0 = b * BS;
    const int nn = min(BS, N - node0);

    STAMP(0);
    int total = 0;
    for (int r0 = 0; r0 < G1; r0 += NTM) {
        int r = r0 + tid0;
        int s = 0, len = 0;
        if (r < G1) {
            s = off[(size_t)b * G1 + r];
            len = off[(size_t)(b + 1) * G1 + r] - s;
        }
        if (r0 == 0) {
            for (int i = tid0; i < 8 * 64; i += NTM) l_wo[i] = wof[i];
        }
        const int ln = tid0 & 63, wv = tid0 >> 6;
        int inc = len;
#pragma unroll
        for (int o = 1; o < 64; o <<= 1) {
            int t = __shfl_up(inc, o, 64);
            if (ln >= o) inc += t;
        }
        if (r0 > 0) __syncthreads();
        if (ln == 63) l_wsum[wv] = inc;
        __syncthreads();
        int pre = 0, tt = 0;
#pragma unroll
        for (int w = 0; w < NWM; ++w) {
            int v = l_wsum[w];
            pre += (w < wv) ? v : 0;
            tt += v;
        }
        if (r < G1) {
            l_rs[r] = s;
            l_rf[r] = total + pre + inc - len;
        }
        total += tt;
    }
    if (tid0 == 0) l_rf[G1] = total;
    STAMP(1);
    const int nchunks = max(1, (total + CAP - 1) / CAP);
    const __amdgpu_buffer_rsrc_t xrsrc =
        __builtin_amdgcn_make_buffer_rsrc((void*)xp, (short)0, (int)((uint32_t)N * 512u), 0x00020000);

#pragma unroll 1
    for (int ck = 0; ck < nchunks; ++ck) {
        const int c0 = ck * CAP;
        const int c1 = min(total, c0 + CAP);
        const int cn = c1 - c0;
        int tid = tid0;
        asm volatile("" : "+v"(tid));
        __syncthreads();
        for (int r = tid >> 3; r < G1; r += NTM / 8) {
            int f0 = l_rf[r], f1 = l_rf[r + 1];
            int base = r * CH + l_rs[r] - f0;
            int lo = max(f0, c0), hi = min(f1, c1);
            for (int f = lo + (tid & 7); f < hi; f += 8) {
                l_map[f - c0] = base + f;
                l_base[f - c0] = (uint32_t)r;
            }
        }
        const int ncell = nn * NKB;
        for (int i = tid; i < ncell; i += NTM) l_cell[i] = 0;
        if (tid == 0) *l_ctr = 0;
        __syncthreads();
        STAMP(2);
        uint2 ra[RPT], rb[RPT], rc[RPT];
        uint32_t key[RPT];
        int cell[RPT];
#pragma unroll
        for (int i = 0; i < RPT; ++i) {
            int f = tid + i * NTM;
            ra[i] = make_uint2(0u, 0u);
            rb[i] = make_uint2(0u, 0u);
            rc[i] = make_uint2(0u, 0u);
            key[i] = 0;
            if (f < cn) {
                int gi = l_map[f];
                const uint2* p = reinterpret_cast<const uint2*>(recs + (size_t)gi * 6);
                ra[i] = p[0];
                rb[i] = p[1];
                rc[i] = p[2];
                key[i] = l_base[f];
            }
        }
#pragma unroll
        for (int i = 0; i < RPT; ++i) {
            int f = tid + i * NTM;
            cell[i] = (int)(ra[i].y & 0xffffu) * NKB + (int)(key[i] >> kshift);
            key[i] = key[i] * (uint32_t)CH + (ra[i].y >> 16);
            if (f < cn) atomicAdd(&l_cell[cell[i]], 1);
        }
        __syncthreads();
        STAMP(3);
        {
            int cv[CPT];
            int sum = 0;
#pragma unroll
            for (int k = 0; k < CPT; ++k) {
                int idx = tid * CPT + k;
                cv[k] = (idx < ncell) ? l_cell[idx] : 0;
                sum += cv[k];
            }
            const int ln = tid & 63, wv = tid >> 6;
            int inc = sum;
#pragma unroll
            for (int o = 1; o < 64; o <<= 1) {
                int t = __shfl_up(inc, o, 64);
                if (ln >= o) inc += t;
            }
            if (ln == 63) l_wsum[wv] = inc;
            __syncthreads();
            int pre = inc - sum;
#pragma unroll
            for (int w = 0; w < NWM; ++w) pre += (w < wv) ? l_wsum[w] : 0;
#pragma unroll
            for (int k = 0; k < CPT; ++k) {
                int idx = tid * CPT + k;
                if (idx < ncell) {
                    l_cell[idx] = pre;
                    if ((idx & (NKB - 1)) == 0) l_start[idx / NKB] = pre;
                }
                pre += cv[k];
            }
            if (tid == 0) l_start[nn] = cn;
        }
        __syncthreads();
#pragma unroll
        for (int i = 0; i < RPT; ++i) {
            int f = tid + i * NTM;
            if (f < cn) {
                int pos = atomicAdd(&l_cell[cell[i]], 1);
                l_key[pos] = key[i];
            }
        }
        __syncthreads();
        STAMP(4);
#pragma unroll
        for (int i = 0; i < RPT; ++i) {
            int f = tid + i * NTM;
            if (f < cn) {
                int s0 = (cell[i] > 0) ? l_cell[cell[i] - 1] : 0;
                int s1 = l_cell[cell[i]];
                int rank = 0;
                for (int k = s0; k < s1; ++k) rank += (l_key[k] < key[i]) ? 1 : 0;
                int pos = s0 + rank;
                l_src[pos] = ra[i].x;
                l_sh[pos] = make_float4(bf_lo(rb[i].x), bf_hi(rb[i].x), bf_lo(rb[i].y), bf_hi(rb[i].y));
                l_ea[pos] = rc[i];
            }
        }
        __syncthreads();
        STAMP(5);
        if (nchunks == 1)
            node_loop<false>(smem, xrsrc, cg, w_r, out, accws, node0, nn, tid, true, true);
        else
            node_loop<true>(smem, xrsrc, cg, w_r, out, accws, node0, nn, tid, ck == 0, ck == nchunks - 1);
    }
}

extern "C" void kernel_launch(void* const* d_in, const int* in_sizes, int n_in,
                              void* d_out, int out_size, void* d_ws, size_t ws_size,
                              hipStream_t stream) {
    const uint4* x = (const uint4*)d_in[0];
    const int* src = (const int*)d_in[1];
    const int* dst = (const int*)d_in[2];
    const uint2* sh = (const uint2*)d_in[3];
    const uint2* ea = (const uint2*)d_in[4];
    const __hip_bfloat16* cg = (const __hip_bfloat16*)d_in[5];
    const __hip_bfloat16* w_r = (const __hip_bfloat16*)d_in[6];
    const __hip_bfloat16* w_out = (const __hip_bfloat16*)d_in[7];
    __hip_bfloat16* out = (__hip_bfloat16*)d_out;

    const int N = in_sizes[0] / 256;
    const int E = in_sizes[1];

    int G1 = 256;
    while ((long long)G1 * (NTP * EPT) < (long long)E) G1 += 256;
    int CH = (E + G1 - 1) / G1;
    if (CH < 1) CH = 1;
    int BS = (N + TGTNBK - 1) / TGTNBK;
    if (BS < 1) BS = 1;
    if (BS > MAXBS) BS = MAXBS;
    int NBK = (N + BS - 1) / BS;
    if (NBK < 1) NBK = 1;

    unsigned char* ws = (unsigned char*)d_ws;
    size_t xp_bytes = 0;
    uint32_t* recs = (uint32_t*)(ws + xp_bytes);
    size_t recs_bytes = ((size_t)G1 * CH * 24 + 255) & ~(size_t)255;
    int* off = (int*)(ws + xp_bytes + recs_bytes);
    size_t off_bytes = (((size_t)(NBK + 1) * G1 * 4) + 255) & ~(size_t)255;
    uint4* wof = (uint4*)(ws + xp_bytes + recs_bytes + off_bytes);
    float* accws = (float*)(ws + xp_bytes + recs_bytes + off_bytes + 8192);

    size_t lds_p = (size_t)MAXNBK * 4 + 128 + (size_t)CH * 24;
    if (lds_p < 16384) lds_p = 16384;
    (void)hipFuncSetAttribute((const void*)k_prep, hipFuncAttributeMaxDynamicSharedMemorySize, (int)lds_p);
    size_t lds_m = (size_t)LDS_CELL + (size_t)(BS * NKB + 4) * 4 + (size_t)(2 * G1 + 1) * 4;
    (void)hipFuncSetAttribute((const void*)k_main, hipFuncAttributeMaxDynamicSharedMemorySize, (int)lds_m);

    k_prep<<<G1, NTP, lds_p, stream>>>(src, dst, sh, ea, w_out, wof, E, CH, G1, BS, NBK, recs, off);
    int kshift = 0;
    while (((G1 - 1) >> kshift) >= NKB) ++kshift;
    k_main<<<NBK, NTM, lds_m, stream>>>(x, recs, off, cg, w_r, wof, out, accws, N, CH, G1, BS, kshift);
}
```

```cpp
#include <hip/hip_runtime.h>
#include <hip/hip_bf16.h>
#include <stdint.h>

typedef __attribute__((ext_vector_type(8))) __bf16 bf16x8_t;
typedef __attribute__((ext_vector_type(4))) float f32x4_t;
typedef __attribute__((ext_vector_type(4))) short v4i16_t;
typedef __attribute__((ext_vector_type(4))) unsigned int u32x4_t;
typedef __attribute__((ext_vector_type(2))) unsigned int u32x2_t;

#define NTP 1024
#define EPT 4
#define NTM 1024
#define NWM (NTM / 64)
#define CAP 3520
#define RPT ((CAP + NTM - 1) / NTM)
#define MAXBS 256
#define MAXNBK 1024
#define TGTNBK 256
#define STG_ROW 72

__device__ __forceinline__ float bf_lo(uint32_t v) { return __uint_as_float(v << 16); }
__device__ __forceinline__ float bf_hi(uint32_t v) { return __uint_as_float(v & 0xffff0000u); }

typedef __attribute__((ext_vector_type(2))) float f32x2_t;
typedef __attribute__((ext_vector_type(2))) __bf16 bf16x2_t;
__device__ __forceinline__ uint32_t pack_bf16(float a, float b) {
    f32x2_t v = {a, b};
    bf16x2_t r = __builtin_convertvector(v, bf16x2_t);
    return __builtin_bit_cast(uint32_t, r);
}

template <int NT>
__device__ __forceinline__ int block_excl_scan(int v, int* wsum, int* total) {
    const int lane = threadIdx.x & 63, wid = threadIdx.x >> 6;
    int inc = v;
#pragma unroll
    for (int o = 1; o < 64; o <<= 1) {
        int t = __shfl_up(inc, o, 64);
        if (lane >= o) inc += t;
    }
    __syncthreads();
    if (lane == 63) wsum[wid] = inc;
    __syncthreads();
    if (wid == 0) {
        int w = (lane < NT / 64) ? wsum[lane] : 0;
        int winc = w;
#pragma unroll
        for (int o = 1; o < 64; o <<= 1) {
            int t = __shfl_up(winc, o, 64);
            if (lane >= o) winc += t;
        }
        if (lane < NT / 64) wsum[lane] = winc - w;
        if (lane == NT / 64 - 1) wsum[NT / 64] = winc;
    }
    __syncthreads();
    int res = wsum[wid] + inc - v;
    *total = wsum[NT / 64];
    return res;
}

__global__ __launch_bounds__(NTP) void k_prep(
    const int* __restrict__ src, const int* __restrict__ dst,
    const uint2* __restrict__ sh, const uint2* __restrict__ ea,
    const __hip_bfloat16* __restrict__ w_out, uint4* __restrict__ wof,
    int E, int CH, int G1, int BS, int NBK,
    uint32_t* __restrict__ recs, int* __restrict__ off) {
    extern __shared__ __attribute__((aligned(16))) unsigned char smem[];
    const int tid = threadIdx.x;
    if (blockIdx.x == 0 && tid < 512) {
        const uint16_t* wo = reinterpret_cast<const uint16_t*>(w_out);
        int tk = tid >> 6, l = tid & 63;
        int t = tk >> 1, ks = tk & 1;
        int d = ((l & 15) >> 2) * 16 + 4 * t + (l & 3);
        int c0 = 32 * ks + 8 * (l >> 4);
        uint32_t v[4];
#pragma unroll
        for (int j = 0; j < 4; ++j)
            v[j] = (uint32_t)wo[(c0 + 2 * j) * 64 + d] | ((uint32_t)wo[(c0 + 2 * j + 1) * 64 + d] << 16);
        wof[tid] = make_uint4(v[0], v[1], v[2], v[3]);
    }
    int* hist = reinterpret_cast<int*>(smem);
    int* wsum = hist + MAXNBK;
    uint32_t* stage = reinterpret_cast<uint32_t*>(smem + MAXNBK * 4 + 128);
    const int blk = blockIdx.x;
    const int e0 = blk * CH;
    const int e1 = min(E, e0 + CH);
    const int cnt = max(0, e1 - e0);

    int dv[EPT], sv[EPT];
    uint2 shv[EPT], eav[EPT];
#pragma unroll
    for (int i = 0; i < EPT; ++i) {
        int j = tid + i * NTP;
        dv[i] = 0; sv[i] = 0;
        shv[i] = make_uint2(0u, 0u);
        eav[i] = make_uint2(0u, 0u);
        if (j < cnt) {
            dv[i] = dst[e0 + j];
            sv[i] = src[e0 + j];
            shv[i] = sh[e0 + j];
            eav[i] = ea[e0 + j];
        }
    }
    for (int i = tid; i < NBK; i += NTP) hist[i] = 0;
    __syncthreads();

    int bk[EPT], dl[EPT];
#pragma unroll
    for (int i = 0; i < EPT; ++i) {
        int j = tid + i * NTP;
        int b = dv[i] / BS;
        bk[i] = b;
        dl[i] = dv[i] - b * BS;
        if (j < cnt) atomicAdd(&hist[b], 1);
    }
    __syncthreads();
    int hv = (tid < NBK) ? hist[tid] : 0;
    int total;
    int st = block_excl_scan<NTP>(hv, wsum, &total);
    if (tid < NBK) {
        hist[tid] = st;
        off[(size_t)tid * G1 + blk] = st;
    }
    if (tid == 0) off[(size_t)NBK * G1 + blk] = total;
    __syncthreads();
#pragma unroll
    for (int i = 0; i < EPT; ++i) {
        int j = tid + i * NTP;
        if (j < cnt) {
            int pos = atomicAdd(&hist[bk[i]], 1);
            uint2* p = reinterpret_cast<uint2*>(stage + (size_t)pos * 6);
            p[0] = make_uint2((uint32_t)sv[i], ((uint32_t)j << 16) | (uint32_t)dl[i]);
            p[1] = shv[i];
            p[2] = eav[i];
        }
    }
    __syncthreads();
    uint2* gout = reinterpret_cast<uint2*>(recs + (size_t)blk * CH * 6);
    const uint2* lin = reinterpret_cast<const uint2*>(stage);
    for (int i = tid; i < cnt * 3; i += NTP) gout[i] = lin[i];
}

struct Cur {
    int nl, j0, je;
};

#define NKB 16
#define CPT ((MAXBS * NKB + NTM - 1) / NTM)
#define LDS_SH 0
#define LDS_EA (LDS_SH + (CAP + 8) * 16)
#define LDS_SRC (LDS_EA + (CAP + 8) * 8)
#define LDS_WO (LDS_SRC + (CAP + 8) * 4)
#define LDS_KEY (LDS_WO + 8192)
#define LDS_MAP LDS_SH
#define LDS_BASE (LDS_SH + CAP * 4)
#define LDS_STG LDS_KEY
#define LDS_START (LDS_KEY + CAP * 4)
#define LDS_WSUM (LDS_START + (MAXBS + 4) * 4)
#define LDS_CELL (LDS_WSUM + 32 * 4)

struct Frag {
    u32x2_t r[4];
};

template <bool MULTI>
__device__ __forceinline__ void node_loop(
    unsigned char* smem, const __amdgpu_buffer_rsrc_t xrsrc,
    const __hip_bfloat16* __restrict__ cg, const __hip_bfloat16* __restrict__ w_r,
    __hip_bfloat16* __restrict__ out, float* __restrict__ accws,
    int node0, int nn, int tid, bool first, bool last) {
    const float4* l_sh = reinterpret_cast<const float4*>(smem + LDS_SH);
    const uint16_t* l_ea = reinterpret_cast<const uint16_t*>(smem + LDS_EA);
    const uint32_t* l_src = reinterpret_cast<const uint32_t*>(smem + LDS_SRC);
    const uint4* l_wo = reinterpret_cast<const uint4*>(smem + LDS_WO);
    const int* l_start = reinterpret_cast<const int*>(smem + LDS_START);
    int* l_ctr = reinterpret_cast<int*>(smem + LDS_WSUM) + 24;
    const int lane = tid & 63, wid = __builtin_amdgcn_readfirstlane(tid >> 6);

    const int gq = lane >> 4;
    const int cl = lane & 15;
    float cgw[4][4];
#pragma unroll
    for (int u = 0; u < 4; ++u)
#pragma unroll
        for (int v = 0; v < 4; ++v) cgw[u][v] = __bfloat162float(cg[(u * 4 + v) * 4 + (lane & 3)]);
    float wrl[4];
#pragma unroll
    for (int t = 0; t < 4; ++t) wrl[t] = __bfloat162float(w_r[gq * 64 + 4 * cl + t]);
    const int cch = 4 * cl + gq;
    const int dsel = cl >> 2;
    uint16_t* stg = reinterpret_cast<uint16_t*>(smem + LDS_STG) + wid * 4 * STG_ROW;

    auto next_node = [&](Cur& c) {
        int v = 0;
        if (lane == 0) v = atomicAdd(l_ctr, 1);
        v = __builtin_amdgcn_readfirstlane(v);
        c.nl = v;
        c.j0 = 0;
        c.je = 0;
        if (v < nn) {
            c.j0 = __builtin_amdgcn_readfirstlane(l_start[v]);
            c.je = __builtin_amdgcn_readfirstlane(l_start[v + 1]);
        }
    };
    auto advance = [&](Cur& c) {
        if (c.nl < nn) {
            c.j0 += 4;
            if (c.j0 >= c.je) next_node(c);
        }
    };
    auto issue = [&](Frag& h, const Cur& c) {
        const int j = c.j0 + gq;
        const uint32_t s = l_src[j];
        uint32_t vo = (j < c.je) ? (s * 512u + (uint32_t)cl * 8u) : 0xFFFFF000u;
#pragma unroll
        for (int u = 0; u < 4; ++u) h.r[u] = __builtin_amdgcn_raw_buffer_load_b64(xrsrc, vo + u * 128, 0, 0);
    };
    f32x4_t acc[4];
#pragma unroll
    for (int t = 0; t < 4; ++t) acc[t] = (f32x4_t){0.f, 0.f, 0.f, 0.f};

    auto process = [&](const Frag& h, const Cur& c) {
        if (c.je > c.j0) {
        const int jr = c.j0 + gq;
        const int j = min(jr, c.je - 1);
        const float4 sv = l_sh[j];
        uint32_t edb = l_ea[j * 4 + dsel];
        const float ed = (jr < c.je) ? __uint_as_float(edb << 16) : 0.f;
        float p[4];
#pragma unroll
        for (int u = 0; u < 4; ++u) {
            float a = cgw[u][0] * sv.x;
            a = fmaf(cgw[u][1], sv.y, a);
            a = fmaf(cgw[u][2], sv.z, a);
            a = fmaf(cgw[u][3], sv.w, a);
            p[u] = a * ed;
        }
        uint32_t phi[2], plo[2];
#pragma unroll
        for (int k = 0; k < 2; ++k) {
            uint32_t hi = pack_bf16(p[2 * k], p[2 * k + 1]);
            float r0 = p[2 * k] - bf_lo(hi);
            float r1 = p[2 * k + 1] - bf_hi(hi);
            phi[k] = hi;
            plo[k] = pack_bf16(r0, r1);
        }
        const v4i16_t ahi = __builtin_bit_cast(v4i16_t, (u32x2_t){phi[0], phi[1]});
        const v4i16_t alo = __builtin_bit_cast(v4i16_t, (u32x2_t){plo[0], plo[1]});
        const v4i16_t b0 = __builtin_bit_cast(v4i16_t, (u32x2_t){__builtin_amdgcn_perm(h.r[1][0], h.r[0][0], 0x05040100u),
                                                                 __builtin_amdgcn_perm(h.r[3][0], h.r[2][0], 0x05040100u)});
        const v4i16_t b1 = __builtin_bit_cast(v4i16_t, (u32x2_t){__builtin_amdgcn_perm(h.r[1][0], h.r[0][0], 0x07060302u),
                                                                 __builtin_amdgcn_perm(h.r[3][0], h.r[2][0], 0x07060302u)});
        const v4i16_t b2 = __builtin_bit_cast(v4i16_t, (u32x2_t){__builtin_amdgcn_perm(h.r[1][1], h.r[0][1], 0x05040100u),
                                                                 __builtin_amdgcn_perm(h.r[3][1], h.r[2][1], 0x05040100u)});
        const v4i16_t b3 = __builtin_bit_cast(v4i16_t, (u32x2_t){__builtin_amdgcn_perm(h.r[1][1], h.r[0][1], 0x07060302u),
                                                                 __builtin_amdgcn_perm(h.r[3][1], h.r[2][1], 0x07060302u)});
        acc[0] = __builtin_amdgcn_mfma_f32_16x16x16bf16_1k(ahi, b0, acc[0], 0, 0, 0);
        acc[1] = __builtin_amdgcn_mfma_f32_16x16x16bf16_1k(ahi, b1, acc[1], 0, 0, 0);
        acc[2] = __builtin_amdgcn_mfma_f32_16x16x16bf16_1k(ahi, b2, acc[2], 0, 0, 0);
        acc[3] = __builtin_amdgcn_mfma_f32_16x16x16bf16_1k(ahi, b3, acc[3], 0, 0, 0);
        acc[0] = __builtin_amdgcn_mfma_f32_16x16x16bf16_1k(alo, b0, acc[0], 0, 0, 0);
        acc[1] = __builtin_amdgcn_mfma_f32_16x16x16bf16_1k(alo, b1, acc[1], 0, 0, 0);
        acc[2] = __builtin_amdgcn_mfma_f32_16x16x16bf16_1k(alo, b2, acc[2], 0, 0, 0);
        acc[3] = __builtin_amdgcn_mfma_f32_16x16x16bf16_1k(alo, b3, acc[3], 0, 0, 0);
        }
        if (c.j0 + 4 >= c.je) {
            float v[4][4];
#pragma unroll
            for (int t = 0; t < 4; ++t)
#pragma unroll
                for (int w = 0; w < 4; ++w) {
                    v[t][w] = acc[t][w] * wrl[t];
                    acc[t][w] = 0.f;
                }
            float r1[2][4];
#pragma unroll
            for (int t = 0; t < 2; ++t)
#pragma unroll
                for (int w = 0; w < 4; ++w) {
                    auto r = __builtin_amdgcn_permlane32_swap(__float_as_uint(v[t][w]), __float_as_uint(v[t + 2][w]),
                                                              false, false);
                    r1[t][w] = __uint_as_float(r[0]) + __uint_as_float(r[1]);
                }
            float ag[4];
#pragma unroll
            for (int w = 0; w < 4; ++w) {
                auto r = __builtin_amdgcn_permlane16_swap(__float_as_uint(r1[0][w]), __float_as_uint(r1[1][w]),
                                                          false, false);
                ag[w] = __uint_as_float(r[0]) + __uint_as_float(r[1]);
            }
            if (MULTI) {
                float* ap = accws + (size_t)(node0 + c.nl) * 256 + cch;
                if (!first) {
#pragma unroll
                    for (int w = 0; w < 4; ++w) ag[w] += ap[w * 64];
                }
                if (!last) {
#pragma unroll
                    for (int w = 0; w < 4; ++w) ap[w * 64] = ag[w];
                }
            }
            if (!MULTI || last) {
                uint32_t p01 = pack_bf16(ag[0], ag[1]);
                uint32_t p23 = pack_bf16(ag[2], ag[3]);
                stg[0 * STG_ROW + cch] = (uint16_t)(p01 & 0xffffu);
                stg[1 * STG_ROW + cch] = (uint16_t)(p01 >> 16);
                stg[2 * STG_ROW + cch] = (uint16_t)(p23 & 0xffffu);
                stg[3 * STG_ROW + cch] = (uint16_t)(p23 >> 16);
                __builtin_amdgcn_wave_barrier();
                bf16x8_t bfr[2];
#pragma unroll
                for (int ks = 0; ks < 2; ++ks) {
                    uint4 vv = *reinterpret_cast<const uint4*>(stg + (lane & 3) * STG_ROW + 32 * ks + 8 * gq);
                    bfr[ks] = __builtin_bit_cast(bf16x8_t, vv);
                }
                uint32_t ow[8];
#pragma unroll
                for (int t = 0; t < 4; ++t) {
                    f32x4_t d4 = {0.f, 0.f, 0.f, 0.f};
#pragma unroll
                    for (int ks = 0; ks < 2; ++ks) {
                        bf16x8_t afr = __builtin_bit_cast(bf16x8_t, l_wo[(t * 2 + ks) * 64 + lane]);
                        d4 = __builtin_amdgcn_mfma_f32_16x16x32_bf16(afr, bfr[ks], d4, 0, 0, 0);
                    }
                    ow[2 * t] = pack_bf16(d4[0], d4[1]);
                    ow[2 * t + 1] = pack_bf16(d4[2], d4[3]);
                }
                __builtin_amdgcn_wave_barrier();
                if (cl < 4) {
                    uint4* op = reinterpret_cast<uint4*>(out + ((size_t)(node0 + c.nl) * 4 + cl) * 64 + gq * 16);
                    op[0] = make_uint4(ow[0], ow[1], ow[2], ow[3]);
                    op[1] = make_uint4(ow[4], ow[5], ow[6], ow[7]);
                }
            }
        }
    };

    Frag h0, h1, h2;
    Cur c0, c1, c2;
    next_node(c0);
    issue(h0, c0);
    c1 = c0; advance(c1); issue(h1, c1);
    c2 = c1; advance(c2); issue(h2, c2);
#pragma unroll 1
    while (c0.nl < nn) {
        process(h0, c0); c0 = c2; advance(c0); issue(h0, c0);
        if (c1.nl < nn) process(h1, c1);
        c1 = c0; advance(c1); issue(h1, c1);
        if (c2.nl < nn) process(h2, c2);
        c2 = c1; advance(c2); issue(h2, c2);
    }
}

__global__ __launch_bounds__(NTM) void k_main(
    const uint4* __restrict__ xp, const uint32_t* __restrict__ recs, const int* __restrict__ off,
    const __hip_bfloat16* __restrict__ cg, const __hip_bfloat16* __restrict__ w_r,
    const uint4* __restrict__ wof, __hip_bfloat16* __restrict__ out,
    float* __restrict__ accws, int N, int CH, int G1, int BS, int kshift) {
    extern __shared__ __attribute__((aligned(16))) unsigned char smem[];
    float4* l_sh = reinterpret_cast<float4*>(smem + LDS_SH);
    uint2* l_ea = reinterpret_cast<uint2*>(smem + LDS_EA);
    uint32_t* l_src = reinterpret_cast<uint32_t*>(smem + LDS_SRC);
    uint4* l_wo = reinterpret_cast<uint4*>(smem + LDS_WO);
    int* l_map = reinterpret_cast<int*>(smem + LDS_MAP);
    uint32_t* l_key = reinterpret_cast<uint32_t*>(smem + LDS_KEY);
    uint32_t* l_base = reinterpret_cast<uint32_t*>(smem + LDS_BASE);
    int* l_start = reinterpret_cast<int*>(smem + LDS_START);
    int* l_wsum = reinterpret_cast<int*>(smem + LDS_WSUM);
    int* l_ctr = l_wsum + 24;
    int* l_cell = reinterpret_cast<int*>(smem + LDS_CELL);
    int* l_rs = l_cell + BS * NKB + 4;
    int* l_rf = l_rs + G1;

    const int tid0 = threadIdx.x;
    const int b = blockIdx.x;
    const int node0 = b * BS;
    const int nn = min(BS, N - node0);
    int total = 0;
    for (int r0 = 0; r0 < G1; r0 += NTM) {
        int r = r0 + tid0;
        int s = 0, len = 0;
        if (r < G1) {
            s = off[(size_t)b * G1 + r];
            len = off[(size_t)(b + 1) * G1 + r] - s;
        }
        if (r0 == 0) {
            for (int i = tid0; i < 8 * 64; i += NTM) l_wo[i] = wof[i];
        }
        const int ln = tid0 & 63, wv = tid0 >> 6;
        int inc = len;
#pragma unroll
        for (int o = 1; o < 64; o <<= 1) {
            int t = __shfl_up(inc, o, 64);
            if (ln >= o) inc += t;
        }
        if (r0 > 0) __syncthreads();
        if (ln == 63) l_wsum[wv] = inc;
        __syncthreads();
        int pre = 0, tt = 0;
#pragma unroll
        for (int w = 0; w < NWM; ++w) {
            int v = l_wsum[w];
            pre += (w < wv) ? v : 0;
            tt += v;
        }
        if (r < G1) {
            l_rs[r] = s;
            l_rf[r] = total + pre + inc - len;
        }
        total += tt;
    }
    if (tid0 == 0) l_rf[G1] = total;
    const int nchunks = max(1, (total + CAP - 1) / CAP);
    const __amdgpu_buffer_rsrc_t xrsrc =
        __builtin_amdgcn_make_buffer_rsrc((void*)xp, (short)0, (int)((uint32_t)N * 512u), 0x00020000);

#pragma unroll 1
    for (int ck = 0; ck < nchunks; ++ck) {
        const int c0 = ck * CAP;
        const int c1 = min(total, c0 + CAP);
        const int cn = c1 - c0;
        int tid = tid0;
        asm volatile("" : "+v"(tid));
        __syncthreads();
        for (int r = tid >> 3; r < G1; r += NTM / 8) {
            int f0 = l_rf[r], f1 = l_rf[r + 1];
            int base = r * CH + l_rs[r] - f0;
            int lo = max(f0, c0), hi = min(f1, c1);
            for (int f = lo + (tid & 7); f < hi; f += 8) {
                l_map[f - c0] = base + f;
                l_base[f - c0] = (uint32_t)r;
            }
        }
        const int ncell = nn * NKB;
        for (int i = tid; i < ncell; i += NTM) l_cell[i] = 0;
        if (tid == 0) *l_ctr = 0;
        __syncthreads();
        uint2 ra[RPT], rb[RPT], rc[RPT];
        uint32_t key[RPT];
        int cell[RPT];
#pragma unroll
        for (int i = 0; i < RPT; ++i) {
            int f = tid + i * NTM;
            ra[i] = make_uint2(0u, 0u);
            rb[i] = make_uint2(0u, 0u);
            rc[i] = make_uint2(0u, 0u);
            key[i] = 0;
            if (f < cn) {
                int gi = l_map[f];
                const uint2* p = reinterpret_cast<const uint2*>(recs + (size_t)gi * 6);
                ra[i] = p[0];
                rb[i] = p[1];
                rc[i] = p[2];
                key[i] = l_base[f];
            }
        }
#pragma unroll
        for (int i = 0; i < RPT; ++i) {
            int f = tid + i * NTM;
            cell[i] = (int)(ra[i].y & 0xffffu) * NKB + (int)(key[i] >> kshift);
            key[i] = key[i] * (uint32_t)CH + (ra[i].y >> 16);
            if (f < cn) atomicAdd(&l_cell[cell[i]], 1);
        }
        __syncthreads();
        {
            int cv[CPT];
            int sum = 0;
#pragma unroll
            for (int k = 0; k < CPT; ++k) {
                int idx = tid * CPT + k;
                cv[k] = (idx < ncell) ? l_cell[idx] : 0;
                sum += cv[k];
            }
            const int ln = tid & 63, wv = tid >> 6;
            int inc = sum;
#pragma unroll
            for (int o = 1; o < 64; o <<= 1) {
                int t = __shfl_up(inc, o, 64);
                if (ln >= o) inc += t;
            }
            if (ln == 63) l_wsum[wv] = inc;
            __syncthreads();
            int pre = inc - sum;
#pragma unroll
            for (int w = 0; w < NWM; ++w) pre += (w < wv) ? l_wsum[w] : 0;
#pragma unroll
            for (int k = 0; k < CPT; ++k) {
                int idx = tid * CPT + k;
                if (idx < ncell) {
                    l_cell[idx] = pre;
                    if ((idx & (NKB - 1)) == 0) l_start[idx / NKB] = pre;
                }
                pre += cv[k];
            }
            if (tid == 0) l_start[nn] = cn;
        }
        __syncthreads();
#pragma unroll
        for (int i = 0; i < RPT; ++i) {
            int f = tid + i * NTM;
            if (f < cn) {
                int pos = atomicAdd(&l_cell[cell[i]], 1);
                l_key[pos] = key[i];
            }
        }
        __syncthreads();
#pragma unroll
        for (int i = 0; i < RPT; ++i) {
            int f = tid + i * NTM;
            if (f < cn) {
                int s0 = (cell[i] > 0) ? l_cell[cell[i] - 1] : 0;
                int s1 = l_cell[cell[i]];
                int rank = 0;
                for (int k = s0; k < s1; ++k) rank += (l_key[k] < key[i]) ? 1 : 0;
                int pos = s0 + rank;
                l_src[pos] = ra[i].x;
                l_sh[pos] = make_float4(bf_lo(rb[i].x), bf_hi(rb[i].x), bf_lo(rb[i].y), bf_hi(rb[i].y));
                l_ea[pos] = rc[i];
            }
        }
        __syncthreads();
        if (nchunks == 1)
            node_loop<false>(smem, xrsrc, cg, w_r, out, accws, node0, nn, tid, true, true);
        else
            node_loop<true>(smem, xrsrc, cg, w_r, out, accws, node0, nn, tid, ck == 0, ck == nchunks - 1);
    }
}

extern "C" void kernel_launch(void* const* d_in, const int* in_sizes, int n_in,
                              void* d_out, int out_size, void* d_ws, size_t ws_size,
                              hipStream_t stream) {
    const uint4* x = (const uint4*)d_in[0];
    const int* src = (const int*)d_in[1];
    const int* dst = (const int*)d_in[2];
    const uint2* sh = (const uint2*)d_in[3];
    const uint2* ea = (const uint2*)d_in[4];
    const __hip_bfloat16* cg = (const __hip_bfloat16*)d_in[5];
    const __hip_bfloat16* w_r = (const __hip_bfloat16*)d_in[6];
    const __hip_bfloat16* w_out = (const __hip_bfloat16*)d_in[7];
    __hip_bfloat16* out = (__hip_bfloat16*)d_out;

    const int N = in_sizes[0] / 256;
    const int E = in_sizes[1];

    int G1 = 256;
    while ((long long)G1 * (NTP * EPT) < (long long)E) G1 += 256;
    int CH = (E + G1 - 1) / G1;
    if (CH < 1) CH = 1;
    int BS = (N + TGTNBK - 1) / TGTNBK;
    if (BS < 1) BS = 1;
    if (BS > MAXBS) BS = MAXBS;
    int NBK = (N + BS - 1) / BS;
    if (NBK < 1) NBK = 1;

    unsigned char* ws = (unsigned char*)d_ws;
    uint32_t* recs = (uint32_t*)ws;
    size_t recs_bytes = ((size_t)G1 * CH * 24 + 255) & ~(size_t)255;
    int* off = (int*)(ws + recs_bytes);
    size_t off_bytes = (((size_t)(NBK + 1) * G1 * 4) + 255) & ~(size_t)255;
    uint4* wof = (uint4*)(ws + recs_bytes + off_bytes);
    float* accws = (float*)(ws + recs_bytes + off_bytes + 8192);

    size_t lds_p = (size_t)MAXNBK * 4 + 128 + (size_t)CH * 24;
    if (lds_p < 16384) lds_p = 16384;
    (void)hipFuncSetAttribute((const void*)k_prep, hipFuncAttributeMaxDynamicSharedMemorySize, (int)lds_p);
    size_t lds_m = (size_t)LDS_CELL + (size_t)(BS * NKB + 4) * 4 + (size_t)(2 * G1 + 1) * 4;
    (void)hipFuncSetAttribute((const void*)k_main, hipFuncAttributeMaxDynamicSharedMemorySize, (int)lds_m);

    k_prep<<<G1, NTP, lds_p, stream>>>(src, dst, sh, ea, w_out, wof, E, CH, G1, BS, NBK, recs, off);
    int kshift = 0;
    while (((G1 - 1) >> kshift) >= NKB) ++kshift;
    k_main<<<NBK, NTM, lds_m, stream>>>(x, recs, off, cg, w_r, wof, out, accws, N, CH, G1, BS, kshift);
}
```
